```python
import torch

def _softmax(x, axis=-1):
    return torch.softmax(x, dim=axis)

def _gelu(x):
    return torch.nn.functional.gelu(x, approximate='tanh')

def _silu(x):
    return torch.nn.functional.silu(x)

def _sigmoid(x):
    return torch.sigmoid(x)

def _layernorm(x, axis=-1, eps=1e-05):
    m = x.mean(dim=axis, keepdim=True)
    v = x.var(dim=axis, keepdim=True, unbiased=False)
    return (x - m) / torch.sqrt(v + eps)

def _scatter_add(tgt, idx, src, axis=0):
    return tgt.clone().index_add_(axis, idx, src)

def _segment_sum(src, seg, n):
    out = torch.zeros((n,) + src.shape[1:], dtype=src.dtype)
    return out.index_add_(0, seg, src)

def _pad(x, pw):
    flat = tuple((v for pair in reversed(pw) for v in pair))
    return torch.nn.functional.pad(x, flat)
kernel_name = 'adaLN_cond__gqa_block_cached__swiglu_ffn__decode'

def setup_inputs():

    _w = lambda a: (lambda t: t.to(torch.bfloat16) if t.is_floating_point() else t)(torch.as_tensor(a))

    def _A_setup():
        B, Sq, D = (2, 1, 448)
        Hq, Hkv, Sk, Dh = (16, 8, 8192, 256)
        assert Hq % Hkv == 0, (Hq, Hkv)
        return {'x': _w((lambda *s: torch.randn(*s, dtype=torch.bfloat16) * 0.05)(B, Sq, D)), 'k_cache': _w((lambda *s: torch.randn(*s, dtype=torch.bfloat16) * 3.2)(B, Hkv, Sk, Dh)), 'v_cache': _w((lambda *s: torch.randn(*s, dtype=torch.bfloat16) * 0.4)(B, Hkv, Sk, Dh)), 'wq': _w((lambda *s: torch.randn(*s, dtype=torch.bfloat16) * 3.2)(D, Hq * Dh) * D ** (-0.5)), 'wo': _w((lambda *s: torch.randn(*s, dtype=torch.bfloat16) * 0.4)(Hq * Dh, D) * (Hq * Dh) ** (-0.5))}
    _A_in = _A_setup()

    def _B_setup():
        B, S, D, F = (2, 1, 448, 1792)
        return {'x': _w((lambda *s: torch.randn(*s, dtype=torch.bfloat16) * 0.05)(B, S, D)), 'w_gate': _w((lambda *s: torch.randn(*s, dtype=torch.bfloat16) * 0.05)(D, F)), 'w_up': _w((lambda *s: torch.randn(*s, dtype=torch.bfloat16) * 0.05)(D, F)), 'w_down': _w((lambda *s: torch.randn(*s, dtype=torch.bfloat16) * 0.05)(F, D))}
    _B_in = _B_setup()
    return {'A_x': _A_in['x'], 'A_k_cache': _A_in['k_cache'], 'A_v_cache': _A_in['v_cache'], 'A_wq': _A_in['wq'], 'A_wo': _A_in['wo'], 'B_x': _B_in['x'], 'B_w_gate': _B_in['w_gate'], 'B_w_up': _B_in['w_up'], 'B_w_down': _B_in['w_down'], 'c': _w((lambda *s: torch.randn(*s, dtype=torch.bfloat16) * 0.2)(2, 448) * 8.0), 'w_mod': _w((lambda *s: torch.randn(*s, dtype=torch.bfloat16) * 0.2)(448, 6 * 448) * 448 ** (-0.5) * 8.0)}

def reference(A_x, A_k_cache, A_v_cache, A_wq, A_wo, B_x, B_w_gate, B_w_up, B_w_down, c, w_mod):

    def _A_ref(x, k_cache, v_cache, wq, wo):
        B, Sq, D = x.shape
        Hq, Dh = (16, 256)
        Hkv = k_cache.shape[1]
        rep = Hq // Hkv
        q = (lambda x, *d: x.permute(*d))((x @ wq).reshape(B, Sq, Hq, Dh), 0, 2, 1, 3)
        k = (lambda x, n, axis: torch.repeat_interleave(x, n, dim=axis))(k_cache, rep, axis=1)
        v = (lambda x, n, axis: torch.repeat_interleave(x, n, dim=axis))(v_cache, rep, axis=1)
        scores = torch.einsum('bhqd,bhkd->bhqk', q, k) * Dh ** (-0.5)
        p = _softmax(scores, axis=-1)
        o = torch.einsum('bhqk,bhkd->bhqd', p, v)
        return (lambda x, *d: x.permute(*d))(o, 0, 2, 1, 3).reshape(B, Sq, Hq * Dh) @ wo

    def _B_ref(x, w_gate, w_up, w_down):
        g = _silu(x @ w_gate)
        u = x @ w_up
        return g * u @ w_down
    x = A_x
    mod = (_silu(c) @ w_mod).reshape(c.shape[0], 6, 1, -1)
    sa, ca, ga, sb, cb, gb = (mod[:, 0].clone(), mod[:, 1].clone(), mod[:, 2].clone(), mod[:, 3].clone(), mod[:, 4].clone(), mod[:, 5].clone())
    h = _layernorm(x, axis=-1) * (1 + ca) + sa
    x = x + ga * _A_ref(h, A_k_cache, A_v_cache, A_wq, A_wo)
    h = _layernorm(x, axis=-1) * (1 + cb) + sb
    x = x + gb * _B_ref(h, B_w_gate, B_w_up, B_w_down)
    return x
```

```cpp
#include <hip/hip_runtime.h>
#include <hip/hip_bf16.h>
#include <cstdint>

#define ROUND_SCORES 0
#define MIMIC 1

typedef unsigned short u16;
typedef unsigned v4u __attribute__((ext_vector_type(4)));
typedef __bf16 bf16x2 __attribute__((ext_vector_type(2)));

constexpr int B = 2, D = 448, HQ = 16, HKV = 8, SK = 8192, DH = 256, F = 1792;
constexpr int NSPLIT = 16, KEYS = SK / NSPLIT;

__device__ __forceinline__ float bf2f(u16 v) { return __uint_as_float((unsigned)v << 16); }
__device__ __forceinline__ float rbf_always(float f) { return (float)(__bf16)f; }
__device__ __forceinline__ float rbf(float f) { return MIMIC ? (float)(__bf16)f : f; }
__device__ __forceinline__ u16 f2bf(float f) { __bf16 b = (__bf16)f; return __builtin_bit_cast(u16, b); }
__device__ __forceinline__ float silu(float x) { return x / (1.f + __expf(-x)); }
__device__ __forceinline__ float dot2(unsigned a, unsigned b, float acc) {
  return __builtin_amdgcn_fdot2_f32_bf16(__builtin_bit_cast(bf16x2, a), __builtin_bit_cast(bf16x2, b), acc, false);
}

__device__ void gemv64(const float* sin  , int K, const u16* W, int N, int n0, float* red, float& o0, float& o1) {
  const int t = threadIdx.x, c = t & 63, g = t >> 6;
  float a0 = 0.f, a1 = 0.f;
  for (int k = g; k < K; k += 4) { float w = bf2f(W[(size_t)k * N + n0 + c]); a0 += sin[k] * w; a1 += sin[K + k] * w; }
  red[t] = a0; red[256 + t] = a1; __syncthreads();
  o0 = red[c] + red[64 + c] + red[128 + c] + red[192 + c];
  o1 = red[256 + c] + red[320 + c] + red[384 + c] + red[448 + c];
  __syncthreads();
}

__global__ void k_mod(const u16* c, const u16* wmod, float* mod) {
  __shared__ float s[2 * D]; __shared__ float red[512];
  for (int i = threadIdx.x; i < 2 * D; i += 256) s[i] = rbf(silu(bf2f(c[i])));
  __syncthreads();
  float o0, o1; gemv64(s, D, wmod, 6 * D, blockIdx.x * 64, red, o0, o1);
  if (threadIdx.x < 64) { mod[blockIdx.x * 64 + threadIdx.x] = rbf(o0); mod[6 * D + blockIdx.x * 64 + threadIdx.x] = rbf(o1); }
}

__device__ void ln_mod(const float* x  , const float* mod, int si, int ci, float* h  ) {
  __shared__ float st[4];
  if (threadIdx.x < 2) {
    int b = threadIdx.x; float m = 0.f; for (int i = 0; i < D; ++i) m += x[b * D + i]; m /= D;
    float v = 0.f; for (int i = 0; i < D; ++i) { float d = x[b * D + i] - m; v += d * d; } v /= D;
    st[2 * b] = rbf(m); st[2 * b + 1] = rbf(sqrtf(rbf(rbf(v) + 1e-5f)));
  }
  __syncthreads();
  for (int i = threadIdx.x; i < 2 * D; i += blockDim.x) {
    int b = i / D, j = i % D;
    float n = rbf(rbf(x[i] - st[2 * b]) / st[2 * b + 1]);
    h[i] = rbf(rbf(n * rbf(1.f + mod[b * 6 * D + ci * D + j])) + mod[b * 6 * D + si * D + j]);
  }
  __syncthreads();
}

__global__ void k_q(const u16* x, const float* mod, const u16* wq, u16* q) {
  __shared__ float sx[2 * D], h[2 * D]; __shared__ float red[512];
  for (int i = threadIdx.x; i < 2 * D; i += 256) sx[i] = bf2f(x[i]);
  __syncthreads();
  ln_mod(sx, mod, 0, 1, h);
  float o0, o1; gemv64(h, D, wq, HQ * DH, blockIdx.x * 64, red, o0, o1);
  if (threadIdx.x < 64) { q[blockIdx.x * 64 + threadIdx.x] = f2bf(o0); q[HQ * DH + blockIdx.x * 64 + threadIdx.x] = f2bf(o1); }
}

__global__ void __launch_bounds__(512) k_attn(const u16* q, const u16* K, const u16* V, float* pml, float* po) {
  __shared__ float sc[2][KEYS]; __shared__ float acc[8][2][DH]; __shared__ float smax[2], ssum[2];
  const int s = blockIdx.x, hkv = blockIdx.y, b = blockIdx.z;
  const int t = threadIdx.x, lane = t & 63, w = t >> 6, r = lane >> 5, c = lane & 31;
  const size_t base = ((size_t)(b * HKV + hkv) * SK + (size_t)s * KEYS + w * 64) * DH;
  v4u q0 = *(const v4u*)(q + (size_t)b * HQ * DH + (2 * hkv) * DH + 8 * c);
  v4u q1 = *(const v4u*)(q + (size_t)b * HQ * DH + (2 * hkv + 1) * DH + 8 * c);
  for (int i = 0; i < 32; ++i) {
    v4u k = *(const v4u*)(K + base + (size_t)(2 * i + r) * DH + 8 * c);
    float d0 = 0.f, d1 = 0.f;
    for (int j = 0; j < 4; ++j) { d0 = dot2(k[j], q0[j], d0); d1 = dot2(k[j], q1[j], d1); }
    for (int o = 16; o >= 1; o >>= 1) { d0 += __shfl_xor(d0, o); d1 += __shfl_xor(d1, o); }
    if (c == 0) {
      if (ROUND_SCORES) { d0 = rbf_always(d0); d1 = rbf_always(d1); }
      sc[0][w * 64 + 2 * i + r] = d0 * 0.0625f; sc[1][w * 64 + 2 * i + r] = d1 * 0.0625f; }
  }
  __syncthreads();
  if (w < 2) {
    float m = -INFINITY; for (int i = lane; i < KEYS; i += 64) m = fmaxf(m, sc[w][i]);
    for (int o = 32; o >= 1; o >>= 1) m = fmaxf(m, __shfl_xor(m, o));
    float l = 0.f; for (int i = lane; i < KEYS; i += 64) { float p = __expf(sc[w][i] - m); sc[w][i] = p; l += p; }
    for (int o = 32; o >= 1; o >>= 1) l += __shfl_xor(l, o);
    if (lane == 0) { smax[w] = m; ssum[w] = l; }
  }
  __syncthreads();
  float a0[8] = {0}, a1[8] = {0};
  for (int i = 0; i < 32; ++i) {
    v4u v = *(const v4u*)(V + base + (size_t)(2 * i + r) * DH + 8 * c);
    float p0 = sc[0][w * 64 + 2 * i + r], p1 = sc[1][w * 64 + 2 * i + r];
    for (int j = 0; j < 4; ++j) {
      float lo = __uint_as_float(v[j] << 16), hi = __uint_as_float(v[j] & 0xffff0000u);
      a0[2 * j] += p0 * lo; a0[2 * j + 1] += p0 * hi; a1[2 * j] += p1 * lo; a1[2 * j + 1] += p1 * hi; }
  }
  for (int j = 0; j < 8; ++j) { a0[j] += __shfl_xor(a0[j], 32); a1[j] += __shfl_xor(a1[j], 32); }
  if (r == 0) for (int j = 0; j < 8; ++j) { acc[w][0][8 * c + j] = a0[j]; acc[w][1][8 * c + j] = a1[j]; }
  __syncthreads();
  { int h = t >> 8, d = t & 255; float o = 0.f; for (int i = 0; i < 8; ++i) o += acc[i][h][d];
    size_t row = (size_t)(b * HQ + 2 * hkv + h) * NSPLIT + s;
    po[row * DH + d] = o; if (d == 0) { pml[2 * row] = smax[h]; pml[2 * row + 1] = ssum[h]; } }
}

__global__ void k_comb(const float* pml, const float* po, float* o) {
  int row = blockIdx.x, d = threadIdx.x;
  float M = -INFINITY; for (int s = 0; s < NSPLIT; ++s) M = fmaxf(M, pml[2 * (row * NSPLIT + s)]);
  float L = 0.f, a = 0.f;
  for (int s = 0; s < NSPLIT; ++s) { float w = __expf(pml[2 * (row * NSPLIT + s)] - M); L += w * pml[2 * (row * NSPLIT + s) + 1]; a += w * po[(size_t)(row * NSPLIT + s) * DH + d]; }
  o[row * DH + d] = rbf(a / L);
}

__global__ void k_wo(const float* o, const u16* wo, const u16* x, const float* mod, float* x1) {
  __shared__ float s[2 * HQ * DH]; __shared__ float red[512];
  for (int i = threadIdx.x; i < 2 * HQ * DH; i += 256) s[i] = o[i];
  __syncthreads();
  float o0, o1; gemv64(s, HQ * DH, wo, D, blockIdx.x * 64, red, o0, o1);
  if (threadIdx.x < 64) { int n = blockIdx.x * 64 + threadIdx.x;
    x1[n] = rbf(bf2f(x[n]) + rbf(mod[2 * D + n] * rbf(o0)));
    x1[D + n] = rbf(bf2f(x[D + n]) + rbf(mod[6 * D + 2 * D + n] * rbf(o1))); }
}

__global__ void k_gu(const float* x1, const float* mod, const u16* wg, const u16* wu, float* act) {
  __shared__ float sx[2 * D], h[2 * D]; __shared__ float red[512];
  for (int i = threadIdx.x; i < 2 * D; i += 256) sx[i] = x1[i];
  __syncthreads();
  ln_mod(sx, mod, 3, 4, h);
  float g0, g1, u0, u1; gemv64(h, D, wg, F, blockIdx.x * 64, red, g0, g1); gemv64(h, D, wu, F, blockIdx.x * 64, red, u0, u1);
  if (threadIdx.x < 64) { int n = blockIdx.x * 64 + threadIdx.x;
    act[n] = rbf(rbf(silu(rbf(g0))) * rbf(u0)); act[F + n] = rbf(rbf(silu(rbf(g1))) * rbf(u1)); }
}

__global__ void k_down(const float* act, const u16* wd, const float* x1, const float* mod, u16* out) {
  __shared__ float s[2 * F]; __shared__ float red[512];
  for (int i = threadIdx.x; i < 2 * F; i += 256) s[i] = act[i];
  __syncthreads();
  float o0, o1; gemv64(s, F, wd, D, blockIdx.x * 64, red, o0, o1);
  if (threadIdx.x < 64) { int n = blockIdx.x * 64 + threadIdx.x;
    out[n] = f2bf(x1[n] + rbf(mod[5 * D + n] * rbf(o0)));
    out[D + n] = f2bf(x1[D + n] + rbf(mod[6 * D + 5 * D + n] * rbf(o1))); }
}

extern "C" void kernel_launch(void* const* d_in, const int* in_sizes, int n_in, void* d_out, int out_size, void* d_ws, size_t ws_size, hipStream_t stream) {
  const u16* x = (const u16*)d_in[0]; const u16* K = (const u16*)d_in[1]; const u16* V = (const u16*)d_in[2];
  const u16* wq = (const u16*)d_in[3]; const u16* wo = (const u16*)d_in[4];
  const u16* wg = (const u16*)d_in[6]; const u16* wu = (const u16*)d_in[7]; const u16* wd = (const u16*)d_in[8];
  const u16* c = (const u16*)d_in[9]; const u16* wmod = (const u16*)d_in[10];
  char* ws = (char*)d_ws;
  float* mod = (float*)ws; u16* q = (u16*)(ws + 65536); float* pml = (float*)(ws + 131072); float* o = (float*)(ws + 196608);
  float* x1 = (float*)(ws + 262144); float* act = (float*)(ws + 327680); float* po = (float*)(ws + 1048576);
  k_mod<<<6 * D / 64, 256, 0, stream>>>(c, wmod, mod);
  k_q<<<HQ * DH / 64, 256, 0, stream>>>(x, mod, wq, q);
  k_attn<<<dim3(NSPLIT, HKV, B), 512, 0, stream>>>(q, K, V, pml, po);
  k_comb<<<B * HQ, DH, 0, stream>>>(pml, po, o);
  k_wo<<<D / 64, 256, 0, stream>>>(o, wo, x, mod, x1);
  k_gu<<<F / 64, 256, 0, stream>>>(x1, mod, wg, wu, act);
  k_down<<<D / 64, 256, 0, stream>>>(act, wd, x1, mod, (u16*)d_out);
}
```

```cpp
#include <hip/hip_runtime.h>
#include <hip/hip_bf16.h>
#include <cstdint>

#define DIAG 0

typedef unsigned short u16;
typedef unsigned long long u64;
typedef unsigned v4u __attribute__((ext_vector_type(4)));
typedef unsigned v2u __attribute__((ext_vector_type(2)));
typedef float v4f __attribute__((ext_vector_type(4)));
typedef float v2f __attribute__((ext_vector_type(2)));
typedef __bf16 bf16x2 __attribute__((ext_vector_type(2)));
#define LAS __attribute__((address_space(3)))
#define GAS __attribute__((address_space(1)))
typedef __amdgpu_buffer_rsrc_t rsrc_t;

constexpr int D = 448, HQ = 16, HKV = 8, SK = 8192, DH = 256, FF = 1792;
constexpr int NWG = 256, NT = 768;
constexpr int N1 = 128;
constexpr int N2 = 28;
constexpr int NCG = 56;
constexpr int OFF_EPOCH = 0;
constexpr int OFF_MOD = 1024;
constexpr int OFF_Q = 24576;
constexpr int OFF_X1 = 57344;
constexpr int OFF_ML = 65536;
constexpr int OFF_PO = 131072;
constexpr int OFF_Y1 = OFF_PO + 1048576;
constexpr int OFF_Y2 = OFF_Y1 + NCG * N1 * 128;
constexpr int WS_BYTES = OFF_Y2 + NCG * N2 * 128;
constexpr int OFF_DIAG = 8 << 20;
constexpr int AUX_NT = 2, AUX_SC1 = 16;
constexpr unsigned SPIN_CAP = 1u << 20;
constexpr int OOB = 0x7ffffff0;

#define TD 3
#define WIN 4
#ifndef NVL
#define NVL 12
#endif
#define BAR_AT 3
#define MPOLL_AT 13
#define QPOLL_AT 32
#define ML_AT 11
#define QDIV 10
#define CODEPF 1
#define FPF 0
#define DEARLY 1
#define LSLEEP 1
#define PSLEEP 8

constexpr int L_FLAG = 0;
constexpr int L_Q = 64;
constexpr int L_ML = 1280;
constexpr int L_FW = 1408;
constexpr int L_ST = 1536;
constexpr int L_S1 = 1664;
constexpr int L_RED = 1792;
constexpr int L_H = 2048;
constexpr int L_ACT = 4096;
constexpr int L_O = 8192;
constexpr int L_P = 24576;
constexpr int L_GU = 32768;
constexpr int L_V = 45056;
constexpr int LDS_BYTES = L_V + 8 * NVL * 1024 > 96 * 1024 ? L_V + 8 * NVL * 1024 : 96 * 1024;

__device__ __forceinline__ float bflo(unsigned v) { return __uint_as_float(v << 16); }
__device__ __forceinline__ float bfhi(unsigned v) { return __uint_as_float(v & 0xffff0000u); }
__device__ __forceinline__ unsigned pk2(float lo, float hi) { bf16x2 v = {(__bf16)lo, (__bf16)hi}; return __builtin_bit_cast(unsigned, v); }
__device__ __forceinline__ float rbf(float f) { return (float)(__bf16)f; }
__device__ __forceinline__ float dot2(unsigned a, unsigned b, float acc) {
  return __builtin_amdgcn_fdot2_f32_bf16(__builtin_bit_cast(bf16x2, a), __builtin_bit_cast(bf16x2, b), acc, false);
}
__device__ __forceinline__ unsigned plo(unsigned a, unsigned b) { return __builtin_amdgcn_perm(b, a, 0x05040100u); }
__device__ __forceinline__ unsigned phi(unsigned a, unsigned b) { return __builtin_amdgcn_perm(b, a, 0x07060302u); }
__device__ __forceinline__ float silu(float x) { return x * __builtin_amdgcn_rcpf(1.f + __builtin_amdgcn_exp2f(x * -1.4426950408889634f)); }
template <int CTRL> __device__ __forceinline__ float dpp(float x) {
  return __builtin_bit_cast(float, __builtin_amdgcn_mov_dpp(__builtin_bit_cast(int, x), CTRL, 0xf, 0xf, true));
}
constexpr int XOR1 = 0xB1, XOR2 = 0x4E, XOR7 = 0x141, ROR8 = 0x128, ROR4 = 0x124;
__device__ __forceinline__ float tr32(float a, float b) {
  auto s = __builtin_amdgcn_permlane32_swap(__float_as_uint(a), __float_as_uint(b), false, false);
  return __uint_as_float(s[0]) + __uint_as_float(s[1]);
}
__device__ __forceinline__ float tr16(float a, float b) {
  auto s = __builtin_amdgcn_permlane16_swap(__float_as_uint(a), __float_as_uint(b), false, false);
  return __uint_as_float(s[0]) + __uint_as_float(s[1]);
}
template <int CTRL> __device__ __forceinline__ float trd(float a, float b, bool bit) {
  const float keep = bit ? b : a, send = bit ? a : b;
  return keep + dpp<CTRL>(send);
}
__device__ __forceinline__ float max32(float x) {
  auto s = __builtin_amdgcn_permlane32_swap(__float_as_uint(x), __float_as_uint(x), false, false);
  return fmaxf(__uint_as_float(s[0]), __uint_as_float(s[1]));
}
__device__ __forceinline__ float max16(float x) {
  auto s = __builtin_amdgcn_permlane16_swap(__float_as_uint(x), __float_as_uint(x), false, false);
  return fmaxf(__uint_as_float(s[0]), __uint_as_float(s[1]));
}
__device__ __forceinline__ float wave_sum(float x) {
  x += dpp<XOR1>(x); x += dpp<XOR2>(x); x += dpp<ROR4>(x); x += dpp<ROR8>(x); x = tr16(x, x); return tr32(x, x);
}
__device__ __forceinline__ rsrc_t mkrsrc(const void* p, int bytes) { return __builtin_amdgcn_make_buffer_rsrc((void*)p, 0, bytes, 0x00020000); }
__device__ __forceinline__ v4u ldnt(rsrc_t r, int voff, int soff) { return __builtin_amdgcn_raw_buffer_load_b128(r, voff, soff, AUX_NT); }
__device__ __forceinline__ v4u ldsc(rsrc_t r, int voff) { return __builtin_amdgcn_raw_buffer_load_b128(r, voff, 0, AUX_SC1); }
__device__ __forceinline__ v2u ldsc2(rsrc_t r, int voff) { return __builtin_amdgcn_raw_buffer_load_b64(r, voff, 0, AUX_SC1); }
__device__ __forceinline__ void stsc(rsrc_t r, int voff, v4u v) { __builtin_amdgcn_raw_buffer_store_b128(v, r, voff, 0, AUX_SC1); }
__device__ __forceinline__ void stsc2(rsrc_t r, int voff, v2u v) { __builtin_amdgcn_raw_buffer_store_b64(v, r, voff, 0, AUX_SC1); }
template <int N> __device__ __forceinline__ void waitvm() { asm volatile("s_waitcnt vmcnt(%0)" :: "i"(N) : "memory"); }
#define SB() __builtin_amdgcn_sched_barrier(0)
#define PIN(x) asm volatile("" : "+v"(x))
__device__ __forceinline__ void ldswait() { asm volatile("s_waitcnt lgkmcnt(0)" ::: "memory"); }
__device__ __forceinline__ void wgbar() { SB(); asm volatile("s_waitcnt lgkmcnt(0)" ::: "memory"); __builtin_amdgcn_s_barrier(); asm volatile("" ::: "memory"); SB(); }

template <int N, int SLP = 0> __device__ __forceinline__ void gather(rsrc_t ws, const int (&off)[N], v4u (&g)[N], unsigned tag) {
#pragma unroll 1
  for (unsigned spins = 0; spins < SPIN_CAP; ++spins) {
    bool ok = true;
#pragma unroll
    for (int k = 0; k < N; ++k) g[k] = ldsc(ws, off[k]);
#pragma unroll
    for (int k = 0; k < N; ++k) ok &= ((g[k].y == tag) & (g[k].w == tag)) | (off[k] == OOB);
    if (__all(ok)) break;
    if (SLP) __builtin_amdgcn_s_sleep(SLP);
    asm volatile("" ::: "memory");
  }
}
template <int N> __device__ __forceinline__ void gather2(rsrc_t ws, const int (&off)[N], v2u (&g)[N], unsigned tag) {
#pragma unroll 1
  for (unsigned spins = 0; spins < SPIN_CAP; ++spins) {
    bool ok = true;
#pragma unroll
    for (int k = 0; k < N; ++k) g[k] = ldsc2(ws, off[k]);
#pragma unroll
    for (int k = 0; k < N; ++k) ok &= (g[k].y == tag) | (off[k] == OOB);
    if (__all(ok)) break;
    asm volatile("" ::: "memory");
  }
}

__device__ __forceinline__ void lds_wait_ge(volatile LAS unsigned* word, unsigned want) {
#pragma unroll 1
  for (unsigned spins = 0; spins < (SPIN_CAP << 4); ++spins) { if (*word >= want) break; __builtin_amdgcn_s_sleep(LSLEEP); }
  asm volatile("" ::: "memory");
  SB();
}
typedef short v8s __attribute__((ext_vector_type(8)));
typedef __bf16 v8bf __attribute__((ext_vector_type(8)));
__device__ __forceinline__ v4f mfma(v4u a, v4u b, v4f c) {
  return __builtin_amdgcn_mfma_f32_16x16x32_bf16(__builtin_bit_cast(v8bf, a), __builtin_bit_cast(v8bf, b), c, 0, 0, 0);
}
__device__ __forceinline__ v4u sel4(bool on, v4u v) { return v4u{on ? v.x : 0u, on ? v.y : 0u, on ? v.z : 0u, on ? v.w : 0u}; }
__device__ __forceinline__ void tile_cols(const v4u* src, v4u (&wc)[8]) {
#pragma unroll
  for (int k = 0; k < 4; ++k) {
    wc[2 * k] = v4u{plo(src[0][k], src[1][k]), plo(src[2][k], src[3][k]), plo(src[4][k], src[5][k]), plo(src[6][k], src[7][k])};
    wc[2 * k + 1] = v4u{phi(src[0][k], src[1][k]), phi(src[2][k], src[3][k]), phi(src[4][k], src[5][k]), phi(src[6][k], src[7][k])};
    PIN(wc[2 * k]); PIN(wc[2 * k + 1]);
  }
}
__device__ __forceinline__ v4f tile_gemv(const v4u (&wc)[8], v4u hb, int lane, v4f acc) {
  const int cs = (lane >> 1) & 7;
#pragma unroll
  for (int c = 0; c < 8; ++c) acc = mfma(wc[c], sel4(cs == c, hb), acc);
  return acc;
}

struct Params {
  const u16 *x, *K, *V, *wq, *wo, *wg, *wu, *wd, *c, *wmod;
  u16* out;
  unsigned char* ws;
};

#define STAMP(i) do { } while (0)
#define DIAG_INIT() do { } while (0)
#define DIAG_FLUSH() do { } while (0)

constexpr int issued_before(int n) {
  int a = 32 + NVL + 1 + (n < 32 ? n : 31) / QDIV, b = n + WIN;
  int m = a > b ? a : b;
  return m > 64 ? 64 : m;
}
__device__ __forceinline__ void lds_inc(LAS unsigned char* L, int word, int lane) {
  ldswait();
  if (lane == 0) __hip_atomic_fetch_add((LAS unsigned*)(L + L_FLAG + word * 4), 1u, __ATOMIC_RELAXED, __HIP_MEMORY_SCOPE_WORKGROUP);
  SB();
}
constexpr v4f Z4 = {0.f, 0.f, 0.f, 0.f};

extern "C" __global__ void __launch_bounds__(NT, 3) mk_kernel(Params p) {
  extern __shared__ __attribute__((aligned(16))) unsigned char smem[];
  LAS unsigned char* L = (LAS unsigned char*)smem;
  const int bid = blockIdx.x, t = threadIdx.x, lane = t & 63, w = __builtin_amdgcn_readfirstlane(t >> 6);
  const int x = bid & 7, j = bid >> 3;
  const int hkv = x, b = j >> 4, s = j & 15;
  const int lo16 = lane * 16, g4 = lane >> 4, n16 = lane & 15, hi3 = lane >> 3, lo3 = lane & 7;
  volatile LAS unsigned* flag = (volatile LAS unsigned*)(L + L_FLAG);
  const bool roleF = (j >= 13 && j < 16) || (j == 12 && x >= 4);
  const int f = j >= 13 ? (j - 13) * 8 + x : 20 + x;
  DIAG_INIT();
  STAMP(0);

  if (w < 4) {
    __builtin_amdgcn_s_setprio(2);
    const rsrc_t ws = mkrsrc(p.ws, WS_BYTES);
    unsigned tagv;
#define LOAD_EPOCH() __hip_atomic_load((GAS unsigned*)(p.ws + OFF_EPOCH), __ATOMIC_RELAXED, __HIP_MEMORY_SCOPE_AGENT)
    const bool roleB = j < 8;
    const bool roleA = (j >= 8 && j < 12) || (j == 12 && x < 4) || (j == 16 && (x < 2 || x >= 4));
    const bool roleS = j >= 16;
    const int e = x * 16 + s;
    const bool on = t < D / 2;
    LAS unsigned* H = (LAS unsigned*)(L + L_H + t * 4);
    if (roleA | roleB) {
      const int pitch = roleB ? HQ * DH * 2 : 6 * D * 2;
      const int unit = roleB ? x * 8 + j : j <= 12 ? x + 8 * (j - 8) : x < 2 ? 40 + x : 32 + x;
      const rsrc_t rw = mkrsrc(roleB ? p.wq : p.wmod, D * pitch);
      const int vo = (128 * w + 8 * hi3) * pitch + unit * 128 + lo3 * 16, vo2 = w < 3 ? vo + 64 * pitch : OOB;
      const rsrc_t rx = mkrsrc(roleB ? p.x : p.c, 2 * D * 2);
      const unsigned xo0 = __builtin_amdgcn_raw_buffer_load_b32(rx, on ? t * 4 : OOB, 0, 0), xo1 = __builtin_amdgcn_raw_buffer_load_b32(rx, on ? t * 4 : OOB, D * 2, 0);
      const int xv = (roleB && lane < 56) ? lo16 : OOB;
      const v4u xr0 = __builtin_amdgcn_raw_buffer_load_b128(rx, xv, 0, 0), xr1 = __builtin_amdgcn_raw_buffer_load_b128(rx, xv, D * 2, 0);
      const unsigned ep = LOAD_EPOCH();
      if (t < 8) flag[t] = 0u;
      v4u wt[16];
#pragma unroll
      for (int r = 0; r < 8; ++r) wt[r] = ldnt(rw, vo, r * pitch);
      wgbar();
#pragma unroll
      for (int r = 8; r < 16; ++r) wt[r] = ldnt(rw, vo2, (r & 7) * pitch);
      SB();
      tagv = ep + 1u;
      v4u wc0[8], wc1[8];
      if (roleA) {
        H[0] = pk2(silu(bflo(xo0)), silu(bfhi(xo0))); H[256] = pk2(silu(bflo(xo1)), silu(bfhi(xo1)));
        tile_cols(wt, wc0); tile_cols(wt + 8, wc1);
        STAMP(19);
      } else {
        float s0 = 0.f, s1 = 0.f;
#pragma unroll
        for (int k = 0; k < 4; ++k) { s0 += bflo(xr0[k]) + bfhi(xr0[k]); s1 += bflo(xr1[k]) + bfhi(xr1[k]); }
        const float m0 = wave_sum(s0) * (1.f / D), m1 = wave_sum(s1) * (1.f / D);
        float v0 = 0.f, v1 = 0.f;
#pragma unroll
        for (int k = 0; k < 4; ++k) {
          const float a = bflo(xr0[k]) - m0, c = bfhi(xr0[k]) - m0, d = bflo(xr1[k]) - m1, q = bfhi(xr1[k]) - m1;
          v0 += a * a + c * c; v1 += d * d + q * q;
        }
        if (lane >= 56) { v0 = 0.f; v1 = 0.f; }
        v0 = wave_sum(v0) * (1.f / D); v1 = wave_sum(v1) * (1.f / D);
        const float r0 = __builtin_amdgcn_rcpf(rbf(__builtin_amdgcn_sqrtf(rbf(rbf(v0) + 1e-5f)))), r1 = __builtin_amdgcn_rcpf(rbf(__builtin_amdgcn_sqrtf(rbf(rbf(v1) + 1e-5f))));
        const float mb0 = rbf(m0), mb1 = rbf(m1);
        float n00 = rbf(rbf(bflo(xo0) - mb0) * r0), n01 = rbf(rbf(bfhi(xo0) - mb0) * r0), n10 = rbf(rbf(bflo(xo1) - mb1) * r1), n11 = rbf(rbf(bfhi(xo1) - mb1) * r1);
        PIN(n00); PIN(n01); PIN(n10); PIN(n11);
        tile_cols(wt, wc0); tile_cols(wt + 8, wc1);
        STAMP(24);
        lds_wait_ge(flag + 7, 1u);
        STAMP(26);
        int off[2]; v4u g[2];
        off[0] = on ? OFF_MOD + t * 16 : OOB; off[1] = on ? OFF_MOD + D * 8 + t * 16 : OOB;
        gather<2, PSLEEP>(ws, off, g, tagv);
        STAMP(2);
        const unsigned c0 = pk2(1.f + bflo(g[1].x), 1.f + bflo(g[1].z)), c1 = pk2(1.f + bfhi(g[1].x), 1.f + bfhi(g[1].z));
        const unsigned u0 = pk2(n00 * bflo(c0), n01 * bfhi(c0)), u1 = pk2(n10 * bflo(c1), n11 * bfhi(c1));
        H[0] = on ? pk2(bflo(u0) + bflo(g[0].x), bfhi(u0) + bflo(g[0].z)) : 0u; H[256] = on ? pk2(bflo(u1) + bfhi(g[0].x), bfhi(u1) + bfhi(g[0].z)) : 0u;
      }
      ldswait();
      const LAS v4u* HA = (const LAS v4u*)(L + L_H + (lane & 1) * 1024 + (128 * w + 16 * g4 + (lane & 4) * 2) * 2);
      const v4u ha0 = HA[0], ha1 = HA[8];
      v4f y[8];
#pragma unroll
      for (int c = 0; c < 8; ++c) y[c] = mfma(ha0, wc0[c], Z4);
#pragma unroll
      for (int c = 0; c < 8; ++c) y[c] = mfma(ha1, wc1[c], y[c]);
      if (lane < 8 || (lane >= 24 && lane < 32)) {
        LAS v2f* G = (LAS v2f*)(L + L_GU + w * 1024 + (lane & 8) * 64 + lo3 * 64);
#pragma unroll
        for (int c = 0; c < 8; ++c) G[c] = v2f{y[c].x, y[c].y};
      }
      lds_inc(L, 2, lane);
      if (w == 0) {
        lds_wait_ge(flag + 2, 4u);
        const LAS v4f* G = (const LAS v4f*)(L + L_GU + (lane >> 5) * 2048 + (lane & 31) * 16);
        const v4f a = (G[0] + G[32]) + (G[64] + G[96]);
        const float e0 = roleA ? tr32(a.x, a.z) : tr32(a.x, a.y), e1 = roleA ? tr32(a.y, a.w) : tr32(a.z, a.w);
        const int so = roleA ? OFF_MOD + (64 * unit + 2 * (lane & 31) + (lane >> 5)) * 8 : OFF_Q + ((lane >> 5) * 2048 + unit * 32 + (lane & 31)) * 8;
        stsc2(ws, so, v2u{pk2(e0, e1), tagv});
        SB();
        STAMP(1);
      }
    } else {
      const unsigned ep = LOAD_EPOCH();
      if (t < 8) flag[t] = 0u;
      wgbar();
      tagv = ep + 1u;
    }
    v4u ec0[8], ec1[8];
    const int vd = (w * 16 + n16 < NCG) ? g4 * 8 * D * 2 + w * 256 + n16 * 16 : OOB;
#define LOAD_LAST() { \
      const rsrc_t rw = mkrsrc(roleS ? p.wo + (size_t)e * 32 * D : p.wd + (size_t)f * 64 * D, roleS ? 32 * D * 2 : 64 * D * 2); \
      v4u wt[16]; \
      _Pragma("unroll") for (int k = 0; k < 8; ++k) wt[k] = ldnt(rw, vd, k * D * 2); \
      _Pragma("unroll") for (int k = 8; k < 16; ++k) wt[k] = ldnt(rw, roleS ? OOB : vd, (24 + k) * D * 2); \
      SB(); \
      tile_cols(wt, ec0); tile_cols(wt + 8, ec1); }
    if (roleS || (DEARLY && roleF)) LOAD_LAST()
    const unsigned tag = __builtin_amdgcn_readfirstlane(tagv);
    if (w == 1) {
      u64 pc; unsigned back, l1, l2, l3;
      asm volatile("s_getpc_b64 %0\n.Lmk_anchor:\n\ts_mov_b32 %1, .Lmk_anchor-mk_kernel\n\ts_mov_b32 %2, .Lmk_end1-mk_kernel\n\ts_mov_b32 %3, .Lmk_end2-mk_kernel\n\ts_mov_b32 %4, .Lmk_end3-mk_kernel"
                   : "=s"(pc), "=s"(back), "=s"(l1), "=s"(l2), "=s"(l3));
      const unsigned len = max(l1, max(l2, l3));
      const unsigned co = min((unsigned)(j * 8 + lo3) * 128u, (len - 4u) & ~3u);
      const unsigned cpf = *(const GAS unsigned*)(pc - back + co);
      asm volatile("" :: "v"(cpf));
    }
    if (w == 3) {
      lds_wait_ge(flag + 1, 1u);
      STAMP(25);
      {
        int off[2]; v4u g[2];
        off[0] = OFF_Q + (b * 2048 + hkv * 256) * 8 + lo16; off[1] = off[0] + 1024;
        gather<2, PSLEEP>(ws, off, g, tag);
        *(LAS v2u*)(L + L_Q + lane * 8) = v2u{g[0].x, g[0].z}; *(LAS v2u*)(L + L_Q + 512 + lane * 8) = v2u{g[1].x, g[1].z};
        lds_inc(L, 0, lane);
      }
      STAMP(4);
      lds_wait_ge(flag + 3, 8u);
      const int h = g4 & 1;
      const v2f ml = *(const LAS v2f*)(L + L_ML + (h * 8 + lo3) * 8);
      float M = ml.x;
      M = fmaxf(M, dpp<XOR1>(M)); M = fmaxf(M, dpp<XOR2>(M)); M = fmaxf(M, dpp<XOR7>(M));
      float Ls = __builtin_amdgcn_exp2f(ml.x - M) * ml.y;
      Ls += dpp<XOR1>(Ls); Ls += dpp<XOR2>(Ls); Ls += dpp<XOR7>(Ls);
      const int mo = OFF_ML + (b * 8 + hkv) * 2048 + h * 16;
      stsc(ws, (lane == 0 || lane == 16) ? mo + s * 128 : OOB, v4u{__float_as_uint(M), tag, __float_as_uint(Ls), tag});
      waitvm<0>();
      lds_wait_ge(flag + 6, 1u);
      int off[1]; v4u g[1];
      off[0] = mo + n16 * 128;
      gather<1, PSLEEP>(ws, off, g, tag);
      float G = __uint_as_float(g[0].x);
      G = fmaxf(G, dpp<XOR1>(G)); G = fmaxf(G, dpp<XOR2>(G)); G = fmaxf(G, dpp<ROR4>(G)); G = fmaxf(G, dpp<ROR8>(G));
      float T = __builtin_amdgcn_exp2f(__uint_as_float(g[0].x) - G) * __uint_as_float(g[0].z);
      T += dpp<XOR1>(T); T += dpp<XOR2>(T); T += dpp<ROR4>(T); T += dpp<ROR8>(T);
      if (lane < 32 && !(lane & 8)) *(LAS float*)(L + L_FW + (h * 8 + lo3) * 4) = __builtin_amdgcn_exp2f(ml.x - G) / T;
      lds_inc(L, 5, lane);
      STAMP(3);
    }
    lds_wait_ge(flag + 5, 1u);
    {
      const int h = w >> 1, dp = t & 127;
      v4f f0 = *(const LAS v4f*)(L + L_FW + h * 32), f1 = *(const LAS v4f*)(L + L_FW + h * 32 + 16);
      int po = OFF_PO + ((((2 * hkv + h) * 8 + (dp >> 4)) * 2 + b) * 16 + s) * 256 + (dp & 15) * 16;
      PIN(f0); PIN(f1); PIN(po);
      wgbar();
      const LAS v2f* O = (const LAS v2f*)(L + L_O + h * 1024 + dp * 8);
      v2f v[8];
#pragma unroll
      for (int k = 0; k < 8; ++k) v[k] = O[k * 256];
      SB();
      const v2f o = ((v[0] * f0.x + v[1] * f0.y) + (v[2] * f0.z + v[3] * f0.w)) + ((v[4] * f1.x + v[5] * f1.y) + (v[6] * f1.z + v[7] * f1.w));
      stsc(ws, po, v4u{__float_as_uint(o.x), tag, __float_as_uint(o.y), tag});
      SB();
    }
    STAMP(9);
    const int cg0 = 16 * w + 4 * g4;

    if (roleS) {
      int off[2]; v4u g[2];
      off[0] = OFF_PO + e * 8192 + (lane & 2) * 2048 + (lane >> 2) * 256 + w * 64 + (lane & 1) * 32; off[1] = off[0] + 16;
      int so = cg0 < NCG ? OFF_Y1 + (cg0 * N1 + e) * 128 + n16 * 8 : OOB;
      PIN(so);
      gather<2>(ws, off, g, tag);
      STAMP(10);
      float v = tr16(tr32(__uint_as_float(g[0].x), __uint_as_float(g[1].x)), tr32(__uint_as_float(g[0].z), __uint_as_float(g[1].z)));
      v += dpp<ROR8>(v); v += dpp<ROR4>(v);
      if (n16 < 4) *(LAS u16*)(L + L_S1 + (lane & 2) * 32 + (w * 8 + (lane & 1) * 4 + g4) * 2) = (u16)pk2(v, v);
      wgbar();
      const v4u ob = *(const LAS v4u*)(L + L_S1 + (lane & 1) * 64 + g4 * 16);
      const v4f y = tile_gemv(ec0, ob, lane, Z4);
#pragma unroll
      for (int k = 0; k < 4; ++k) __builtin_amdgcn_raw_buffer_store_b64(v2u{__float_as_uint(y[k]), tag}, ws, so, k * N1 * 128, AUX_SC1);
      STAMP(11);
      asm volatile(".Lmk_end1:");
    } else if (roleF) {
      int om[2]; v4u gm[2];
      om[0] = on ? OFF_MOD + 3 * D * 8 + t * 16 : OOB; om[1] = on ? OFF_MOD + 4 * D * 8 + t * 16 : OOB;
      gm[0] = ldsc(ws, om[0]); gm[1] = ldsc(ws, om[1]);
      wgbar();
      if (!DEARLY) LOAD_LAST()
      if (!__all(((gm[0].y == tag) & (gm[0].w == tag) & (gm[1].y == tag) & (gm[1].w == tag)) | !on)) gather<2>(ws, om, gm, tag);
      float sb[4] = {bflo(gm[0].x), bfhi(gm[0].x), bflo(gm[0].z), bfhi(gm[0].z)};
      float cb[4] = {rbf(1.f + bflo(gm[1].x)), rbf(1.f + bfhi(gm[1].x)), rbf(1.f + bflo(gm[1].z)), rbf(1.f + bfhi(gm[1].z))};
#pragma unroll
      for (int k = 0; k < 4; ++k) { PIN(sb[k]); PIN(cb[k]); }
      int so = cg0 < NCG ? OFF_Y2 + (cg0 * N2 + f) * 128 + n16 * 8 : OOB;
      PIN(so);
      int ox[2]; v4u gx[2];
      ox[0] = on ? OFF_X1 + (t >> 2) * 128 + (t & 3) * 16 : OFF_X1; ox[1] = OFF_X1;
      gather<2>(ws, ox, gx, tag);
      STAMP(15);
      if (!on) gx[0].z = gx[0].x;
      const float xa0 = bflo(gx[0].x), xb0 = bfhi(gx[0].x), xa1 = bflo(gx[0].z), xb1 = bfhi(gx[0].z), pa = bflo(gx[1].x), pb = bfhi(gx[1].x);
      const float da0 = xa0 - pa, db0 = xb0 - pb, da1 = xa1 - pa, db1 = xb1 - pb;
      float u = tr16(tr32(da0 + da1, da0 * da0 + da1 * da1), tr32(db0 + db1, db0 * db0 + db1 * db1));
      u += dpp<XOR1>(u); u += dpp<XOR2>(u); u += dpp<ROR4>(u); u += dpp<ROR8>(u);
      if (n16 == 0) *(LAS float*)(L + L_ST + w * 16 + g4 * 4) = u;
      wgbar();
      const LAS v4f* ST = (const LAS v4f*)(L + L_ST);
      const v4f ss = ((ST[0] + ST[1]) + (ST[2] + ST[3])) * (1.f / D);
      const unsigned vr = pk2(fmaxf(ss.z - ss.x * ss.x, 0.f), fmaxf(ss.w - ss.y * ss.y, 0.f)), mn = pk2(pa + ss.x, pb + ss.y);
      const unsigned ve = pk2(bflo(vr) + 1e-5f, bfhi(vr) + 1e-5f);
      const unsigned sd = pk2(__builtin_amdgcn_sqrtf(bflo(ve)), __builtin_amdgcn_sqrtf(bfhi(ve)));
      const float ra = __builtin_amdgcn_rcpf(bflo(sd)), rb = __builtin_amdgcn_rcpf(bfhi(sd));
      const unsigned ca = pk2(xa0 - bflo(mn), xa1 - bflo(mn)), cc = pk2(xb0 - bfhi(mn), xb1 - bfhi(mn));
      const unsigned na = pk2(bflo(ca) * ra, bfhi(ca) * ra), nb = pk2(bflo(cc) * rb, bfhi(cc) * rb);
      const unsigned ma = pk2(bflo(na) * cb[0], bfhi(na) * cb[2]), mb = pk2(bflo(nb) * cb[1], bfhi(nb) * cb[3]);
      H[0] = pk2(bflo(ma) + sb[0], bfhi(ma) + sb[2]); H[256] = pk2(bflo(mb) + sb[1], bfhi(mb) + sb[3]);
      wgbar();
      wgbar();
      v4f gw[7];
#pragma unroll
      for (int k = 0; k < 7; ++k) gw[k] = *(const LAS v4f*)(L + L_GU + k * 1024 + lo16);
      SB();
      const v4f a = ((gw[0] + gw[1]) + (gw[2] + gw[3])) + ((gw[4] + gw[5]) + gw[6]);
      const unsigned gb = pk2(a.x, a.y), ub = pk2(a.z, a.w), sg = pk2(silu(bflo(gb)), silu(bfhi(gb)));
      const unsigned ac = pk2(bflo(sg) * bflo(ub), bfhi(sg) * bfhi(ub));
      *(LAS u16*)(L + L_ACT + w * 256 + lane * 2) = (u16)ac; *(LAS u16*)(L + L_ACT + w * 256 + 128 + lane * 2) = (u16)(ac >> 16);
      ldswait();
      const LAS v4u* E = (const LAS v4u*)(L + L_ACT + w * 256 + (lane & 1) * 128 + g4 * 16);
      const v4u e0 = E[0], e1 = E[4];
      const v4f y = tile_gemv(ec0, e0, lane, Z4) + tile_gemv(ec1, e1, lane, Z4);
#pragma unroll
      for (int k = 0; k < 4; ++k) __builtin_amdgcn_raw_buffer_store_b64(v2u{__float_as_uint(y[k]), tag}, ws, so, k * N2 * 128, AUX_SC1);
      STAMP(16);
      asm volatile(".Lmk_end2:");
    } else if (j < 7) {
      const int cg = j * 8 + x;
      v2u gm[2] = {{0, 0}, {0, 0}}; unsigned xin0 = 0, xin1 = 0;
      if (w == 0) {
        xin0 = p.x[8 * cg + lo3]; xin1 = p.x[D + 8 * cg + lo3];
        int om[2]; om[0] = OFF_MOD + (2 * D + 8 * cg + lo3) * 8; om[1] = OFF_MOD + (5 * D + 8 * cg + lo3) * 8;
        gather2<2>(ws, om, gm, tag);
        PIN(xin0); PIN(xin1);
      }
      int of[4]; v4u gg[4];
#pragma unroll
      for (int k = 0; k < 4; ++k) of[k] = OFF_Y1 + cg * N1 * 128 + k * 4096 + t * 16;
      gather<4>(ws, of, gg, tag);
      STAMP(12);
      const float a0 = (__uint_as_float(gg[0].x) + __uint_as_float(gg[1].x)) + (__uint_as_float(gg[2].x) + __uint_as_float(gg[3].x));
      const float a1 = (__uint_as_float(gg[0].z) + __uint_as_float(gg[1].z)) + (__uint_as_float(gg[2].z) + __uint_as_float(gg[3].z));
      float v2 = tr32(a0, a1); v2 = tr16(v2, v2); v2 += dpp<ROR8>(v2);
      if ((lane & 24) == 0) *(LAS float*)(L + L_RED + (w * 16 + lo3 * 2 + (lane >> 5)) * 4) = v2;
      wgbar();
      if (w == 0) {
        const LAS v2f* R = (const LAS v2f*)(L + L_RED + lo3 * 8);
        const v2f y1 = (R[0] + R[8]) + (R[16] + R[24]);
        const unsigned yb = pk2(y1.x, y1.y), gy = pk2(bflo(gm[0].x) * bflo(yb), bfhi(gm[0].x) * bfhi(yb));
        const unsigned x1v = pk2(bflo(xin0) + bflo(gy), bflo(xin1) + bfhi(gy));
        stsc2(ws, lane < 8 ? OFF_X1 + cg * 128 + lane * 8 : OOB, v2u{x1v, tag});
        SB();
        STAMP(13);
        int o2[4]; v4u g2[4];
#pragma unroll
        for (int k = 0; k < 4; ++k) o2[k] = (k * 8 + hi3 < N2) ? OFF_Y2 + cg * N2 * 128 + k * 1024 + lo16 : OOB;
        gather<4>(ws, o2, g2, tag);
        STAMP(17);
        const float c0 = (__uint_as_float(g2[0].x) + __uint_as_float(g2[1].x)) + (__uint_as_float(g2[2].x) + __uint_as_float(g2[3].x));
        const float c1 = (__uint_as_float(g2[0].z) + __uint_as_float(g2[1].z)) + (__uint_as_float(g2[2].z) + __uint_as_float(g2[3].z));
        float y2 = tr32(c0, c1); y2 = tr16(y2, y2); y2 += dpp<ROR8>(y2);
        const bool hb = lane >= 32;
        const float res = (hb ? bfhi(x1v) : bflo(x1v)) + rbf((hb ? bfhi(gm[1].x) : bflo(gm[1].x)) * rbf(y2));
        if ((lane & 24) == 0) p.out[(lane >> 5) * D + 8 * cg + lo3] = (u16)pk2(res, res);
        if (cg == 0 && lane == 0) __hip_atomic_store((GAS unsigned*)(p.ws + OFF_EPOCH), tag, __ATOMIC_RELAXED, __HIP_MEMORY_SCOPE_AGENT);
        STAMP(18);
        asm volatile(".Lmk_end3:");
      }
    }
  } else {
    const int sw = w - 4;
    const size_t kvoff = ((size_t)(b * HKV + hkv) * SK + s * 512 + sw * 64) * DH;
    const rsrc_t rK = mkrsrc(p.K + kvoff, 64 * DH * 2), rV = mkrsrc(p.V + kvoff, 64 * DH * 2);
    const int vk = lo3 * 512 + (lane & 8) * 8 + g4 * 16;
    const int vv = g4 * 512 + n16 * 16;
    v4u kv[64];
#define LOADKV(i) kv[i] = (i) < 32 ? ldnt(rK, vk, ((i) >> 2) * 4096 + ((i) & 3) * 128) : ldnt(rV, vv, (((i) - 32) >> 1) * 2048 + ((i) & 1) * 256)
#define ISSUE_TO(FROM, TO) _Pragma("unroll") for (int i_ = (FROM); i_ < (TO); ++i_) { LOADKV(i_); SB(); }
    ISSUE_TO(0, TD)
#pragma unroll
    for (int i = TD; i < 32; ++i) {
      waitvm<TD - 1>(); LOADKV(i); SB();
      if (i == BAR_AT) wgbar();
      if (i == MPOLL_AT) lds_inc(L, 7, lane);
      if (i == QPOLL_AT) lds_inc(L, 1, lane);
    }
    {
      const unsigned lb = (unsigned)(size_t)(L + L_V) + sw * NVL * 1024;
#pragma unroll
      for (int i = 0; i < NVL; ++i) {
        waitvm<TD - 1>();
        unsigned keep;
        asm volatile("s_mov_b32 %0, m0\n\ts_mov_b32 m0, %1\n\ts_nop 0\n\tbuffer_load_dwordx4 %2, %3, %4 offen nt lds\n\ts_mov_b32 m0, %0"
                     : "=&s"(keep) : "s"(lb + i * 1024), "v"(vv), "s"(rV), "s"((i >> 1) * 2048 + (i & 1) * 256) : "memory");
        SB();
        if (32 + i == QPOLL_AT) lds_inc(L, 1, lane);
      }
    }
    STAMP(5);
    lds_wait_ge(flag, 1u);
    v4u qa[4];
#pragma unroll
    for (int st = 0; st < 4; ++st) qa[st] = *(const LAS v4u*)(L + L_Q + (lane & 1) * 512 + st * 128 + (lane & 2) * 32 + g4 * 16);
    STAMP(6);
    float ev0 = 0.f, ev1 = 0.f, ov0 = 0.f, ov1 = 0.f;
    const bool c2 = lane & 8;
#pragma unroll
    for (int tt = 0; tt < 8; ++tt) {
      v4f a = Z4;
#pragma unroll
      for (int st = 0; st < 4; ++st) {
        const int n = tt * 4 + st;
        ISSUE_TO(n == 0 ? 32 + NVL : issued_before(n - 1), issued_before(n))
        a = mfma(qa[st], kv[n], a);
        SB();
      }
      const float v0 = c2 ? a.z : a.x, v1 = c2 ? a.w : a.y;
      const bool mine = g4 == (tt >> 1);
      if (tt & 1) { ov0 = mine ? v0 : ov0; ov1 = mine ? v1 : ov1; } else { ev0 = mine ? v0 : ev0; ev1 = mine ? v1 : ev1; }
    }
    const float sc0 = rbf(trd<ROR8>(ev0, ov0, c2)) * (0.0625f * 1.4426950408889634f);
    const float sc1 = rbf(trd<ROR8>(ev1, ov1, c2)) * (0.0625f * 1.4426950408889634f);
    {
      auto sx = __builtin_amdgcn_permlane32_swap(__float_as_uint(sc0), __float_as_uint(sc1), false, false);
      float m = fmaxf(__uint_as_float(sx[0]), __uint_as_float(sx[1]));
      m = max16(m); m = fmaxf(m, dpp<ROR8>(m)); m = fmaxf(m, dpp<ROR4>(m)); m = fmaxf(m, dpp<XOR2>(m)); m = fmaxf(m, dpp<XOR1>(m));
      const float m0 = __builtin_bit_cast(float, __builtin_amdgcn_readlane(__builtin_bit_cast(int, m), 0));
      const float m1 = __builtin_bit_cast(float, __builtin_amdgcn_readlane(__builtin_bit_cast(int, m), 32));
      const float p0 = __builtin_amdgcn_exp2f(sc0 - m0), p1 = __builtin_amdgcn_exp2f(sc1 - m1);
      float l = tr32(p0, p1);
      l = tr16(l, l); l += dpp<ROR8>(l); l += dpp<ROR4>(l); l += dpp<XOR2>(l); l += dpp<XOR1>(l);
      const unsigned pp = pk2(p0, p1);
      LAS unsigned* P = (LAS unsigned*)(L + L_P + sw * 1024 + (lane & 3) * 64 + (lane >> 2) * 4);
      P[0] = pp & 0xffffu; P[64] = pp << 16; P[128] = pp >> 16; P[192] = pp & 0xffff0000u;
      if ((lane & 31) == 0) *(LAS v2f*)(L + L_ML + ((lane >> 5) * 8 + sw) * 8) = v2f{m, l};
    }
    lds_inc(L, 3, lane);
    waitvm<issued_before(31) - 32 - NVL>();
#pragma unroll
    for (int i = 0; i < NVL; ++i) kv[32 + i] = *(const LAS v4u*)(L + L_V + sw * NVL * 1024 + i * 1024 + lo16);
    v4u pr[4];
#pragma unroll
    for (int k = 0; k < 4; ++k) pr[k] = *(const LAS v4u*)(L + L_P + sw * 1024 + (lane & 8) * 64 + (lane & 1) * 256 + g4 * 64 + k * 16);
    STAMP(7);
    v4f acc0 = Z4, acc1 = Z4;
    const int jq = (lane >> 1) & 3;
    unsigned fpf = 0;
#pragma unroll
    for (int ip = 0; ip < 16; ++ip) {
      ISSUE_TO(issued_before(32 + 2 * ip - 1), issued_before(32 + 2 * ip + 1))
      if (ip == ML_AT) lds_inc(L, 6, lane);
      if (FPF && ip == FPF) {
        const int idx = (j * 8 + sw) * 14 + min(lane, 13), k = idx / 896, r = idx - k * 896, fk = k < 3 ? k * 8 + x : x >= 4 ? 20 + x : x;
        fpf = __builtin_nontemporal_load((const GAS unsigned*)((r < D ? p.wg + (size_t)r * FF : p.wu + (size_t)(r - D) * FF) + fk * 64));
        SB();
      }
      const unsigned pu = pr[ip >> 2][ip & 3];
      const v4u pa = {jq == 0 ? pu : 0u, jq == 1 ? pu : 0u, jq == 2 ? pu : 0u, jq == 3 ? pu : 0u};
      acc0 = mfma(pa, kv[32 + 2 * ip], acc0); acc1 = mfma(pa, kv[33 + 2 * ip], acc1);
      SB();
    }
    LAS v4f* O = (LAS v4f*)(L + L_O + sw * 2048 + (g4 >> 1) * 1024 + n16 * 32 + (g4 & 1) * 16);
    O[0] = acc0; O[32] = acc1;
    STAMP(8);
    wgbar();
    if (FPF) asm volatile("" :: "v"(fpf));
    if (roleF && sw < 7) {
      const GAS v4u* gu = (const GAS v4u*)(((lane & 8) ? p.wu : p.wg) + (size_t)(64 * sw + 8 * g4) * FF + f * 64 + lo3 * 8);
      wgbar();
      v4u wt[16];
#pragma unroll
      for (int k = 0; k < 16; ++k) wt[k] = __builtin_nontemporal_load(gu + ((k >> 3) * 32 + (k & 7)) * (FF / 8));
      SB();
      v4u gc0[8], gc1[8];
      tile_cols(wt, gc0); tile_cols(wt + 8, gc1);
      STAMP(14);
      wgbar();
      wgbar();
      const LAS v4u* HA = (const LAS v4u*)(L + L_H + (lane & 1) * 1024 + (64 * sw + 8 * g4) * 2);
      const v4u h0 = HA[0], h1 = HA[4];
      v4f y[8];
#pragma unroll
      for (int c = 0; c < 8; ++c) y[c] = mfma(h0, gc0[c], Z4);
#pragma unroll
      for (int c = 0; c < 8; ++c) y[c] = mfma(h1, gc1[c], y[c]);
      if (lane < 16) {
        LAS v2f* G = (LAS v2f*)(L + L_GU + sw * 1024 + lo3 * 128 + (lane & 8));
#pragma unroll
        for (int c = 0; c < 8; ++c) G[2 * c] = v2f{y[c].x, y[c].y};
      }
      wgbar();
    }
  }
  DIAG_FLUSH();
}

extern "C" void kernel_launch(void* const* d_in, const int* in_sizes, int n_in, void* d_out, int out_size, void* d_ws, size_t ws_size, hipStream_t stream) {
  Params p;
  p.x = (const u16*)d_in[0]; p.K = (const u16*)d_in[1]; p.V = (const u16*)d_in[2]; p.wq = (const u16*)d_in[3]; p.wo = (const u16*)d_in[4];
  p.wg = (const u16*)d_in[6]; p.wu = (const u16*)d_in[7]; p.wd = (const u16*)d_in[8]; p.c = (const u16*)d_in[9]; p.wmod = (const u16*)d_in[10];
  p.out = (u16*)d_out; p.ws = (unsigned char*)d_ws;
  (void)hipFuncSetAttribute((const void*)mk_kernel, hipFuncAttributeMaxDynamicSharedMemorySize, LDS_BYTES);
  hipLaunchKernelGGL(mk_kernel, dim3(NWG), dim3(NT), LDS_BYTES, stream, p);
}
```

```cpp
#include <hip/hip_runtime.h>
#include <hip/hip_bf16.h>
#include <cstdint>

#define DIAG 0

typedef unsigned short u16;
typedef unsigned long long u64;
typedef unsigned v4u __attribute__((ext_vector_type(4)));
typedef unsigned v2u __attribute__((ext_vector_type(2)));
typedef float v4f __attribute__((ext_vector_type(4)));
typedef float v2f __attribute__((ext_vector_type(2)));
typedef __bf16 bf16x2 __attribute__((ext_vector_type(2)));
#define LAS __attribute__((address_space(3)))
#define GAS __attribute__((address_space(1)))
typedef __amdgpu_buffer_rsrc_t rsrc_t;

constexpr int D = 448, HQ = 16, HKV = 8, SK = 8192, DH = 256, FF = 1792;
constexpr int NWG = 256, NT = 768;
constexpr int N1 = 128;
constexpr int N2 = 28;
constexpr int NCG = 56;
constexpr int OFF_EPOCH = 0;
constexpr int OFF_MOD = 1024;
constexpr int OFF_Q = 24576;
constexpr int OFF_X1 = 57344;
constexpr int OFF_ML = 65536;
constexpr int OFF_PO = 131072;
constexpr int OFF_Y1 = OFF_PO + 1048576;
constexpr int OFF_Y2 = OFF_Y1 + NCG * N1 * 128;
constexpr int WS_BYTES = OFF_Y2 + NCG * N2 * 128;
constexpr int OFF_DIAG = 8 << 20;
constexpr int AUX_NT = 2, AUX_SC1 = 16;
constexpr unsigned SPIN_CAP = 1u << 20;
constexpr int OOB = 0x7ffffff0;

#define TD 3
#define WIN 4
#ifndef NVL
#define NVL 12
#endif
#define BAR_AT 3
#define MPOLL_AT 13
#define QPOLL_AT 32
#define ML_AT 11
#define QDIV 10
#define CODEPF 1
#define FPF 0
#define DEARLY 1
#define LSLEEP 1
#define PSLEEP 8

constexpr int L_FLAG = 0;
constexpr int L_Q = 64;
constexpr int L_ML = 1280;
constexpr int L_FW = 1408;
constexpr int L_ST = 1536;
constexpr int L_S1 = 1664;
constexpr int L_RED = 1792;
constexpr int L_H = 2048;
constexpr int L_ACT = 4096;
constexpr int L_O = 8192;
constexpr int L_P = 24576;
constexpr int L_GU = 32768;
constexpr int L_V = 45056;
constexpr int LDS_BYTES = L_V + 8 * NVL * 1024 > 96 * 1024 ? L_V + 8 * NVL * 1024 : 96 * 1024;

__device__ __forceinline__ float bflo(unsigned v) { return __uint_as_float(v << 16); }
__device__ __forceinline__ float bfhi(unsigned v) { return __uint_as_float(v & 0xffff0000u); }
__device__ __forceinline__ unsigned pk2(float lo, float hi) { bf16x2 v = {(__bf16)lo, (__bf16)hi}; return __builtin_bit_cast(unsigned, v); }
__device__ __forceinline__ float rbf(float f) { return (float)(__bf16)f; }
__device__ __forceinline__ float dot2(unsigned a, unsigned b, float acc) {
  return __builtin_amdgcn_fdot2_f32_bf16(__builtin_bit_cast(bf16x2, a), __builtin_bit_cast(bf16x2, b), acc, false);
}
__device__ __forceinline__ unsigned plo(unsigned a, unsigned b) { return __builtin_amdgcn_perm(b, a, 0x05040100u); }
__device__ __forceinline__ unsigned phi(unsigned a, unsigned b) { return __builtin_amdgcn_perm(b, a, 0x07060302u); }
__device__ __forceinline__ float silu(float x) { return x * __builtin_amdgcn_rcpf(1.f + __builtin_amdgcn_exp2f(x * -1.4426950408889634f)); }
template <int CTRL> __device__ __forceinline__ float dpp(float x) {
  return __builtin_bit_cast(float, __builtin_amdgcn_mov_dpp(__builtin_bit_cast(int, x), CTRL, 0xf, 0xf, true));
}
constexpr int XOR1 = 0xB1, XOR2 = 0x4E, XOR7 = 0x141, ROR8 = 0x128, ROR4 = 0x124;
__device__ __forceinline__ float tr32(float a, float b) {
  auto s = __builtin_amdgcn_permlane32_swap(__float_as_uint(a), __float_as_uint(b), false, false);
  return __uint_as_float(s[0]) + __uint_as_float(s[1]);
}
__device__ __forceinline__ float tr16(float a, float b) {
  auto s = __builtin_amdgcn_permlane16_swap(__float_as_uint(a), __float_as_uint(b), false, false);
  return __uint_as_float(s[0]) + __uint_as_float(s[1]);
}
template <int CTRL> __device__ __forceinline__ float trd(float a, float b, bool bit) {
  const float keep = bit ? b : a, send = bit ? a : b;
  return keep + dpp<CTRL>(send);
}
__device__ __forceinline__ float max32(float x) {
  auto s = __builtin_amdgcn_permlane32_swap(__float_as_uint(x), __float_as_uint(x), false, false);
  return fmaxf(__uint_as_float(s[0]), __uint_as_float(s[1]));
}
__device__ __forceinline__ float max16(float x) {
  auto s = __builtin_amdgcn_permlane16_swap(__float_as_uint(x), __float_as_uint(x), false, false);
  return fmaxf(__uint_as_float(s[0]), __uint_as_float(s[1]));
}
__device__ __forceinline__ float wave_sum(float x) {
  x += dpp<XOR1>(x); x += dpp<XOR2>(x); x += dpp<ROR4>(x); x += dpp<ROR8>(x); x = tr16(x, x); return tr32(x, x);
}
__device__ __forceinline__ rsrc_t mkrsrc(const void* p, int bytes) { return __builtin_amdgcn_make_buffer_rsrc((void*)p, 0, bytes, 0x00020000); }
__device__ __forceinline__ v4u ldnt(rsrc_t r, int voff, int soff) { return __builtin_amdgcn_raw_buffer_load_b128(r, voff, soff, AUX_NT); }
__device__ __forceinline__ v4u ldsc(rsrc_t r, int voff) { return __builtin_amdgcn_raw_buffer_load_b128(r, voff, 0, AUX_SC1); }
__device__ __forceinline__ v2u ldsc2(rsrc_t r, int voff) { return __builtin_amdgcn_raw_buffer_load_b64(r, voff, 0, AUX_SC1); }
__device__ __forceinline__ void stsc(rsrc_t r, int voff, v4u v) { __builtin_amdgcn_raw_buffer_store_b128(v, r, voff, 0, AUX_SC1); }
__device__ __forceinline__ void stsc2(rsrc_t r, int voff, v2u v) { __builtin_amdgcn_raw_buffer_store_b64(v, r, voff, 0, AUX_SC1); }
template <int N> __device__ __forceinline__ void waitvm() { asm volatile("s_waitcnt vmcnt(%0)" :: "i"(N) : "memory"); }
#define SB() __builtin_amdgcn_sched_barrier(0)
#define PIN(x) asm volatile("" : "+v"(x))
__device__ __forceinline__ void ldswait() { asm volatile("s_waitcnt lgkmcnt(0)" ::: "memory"); }
__device__ __forceinline__ void wgbar() { SB(); asm volatile("s_waitcnt lgkmcnt(0)" ::: "memory"); __builtin_amdgcn_s_barrier(); asm volatile("" ::: "memory"); SB(); }

template <int N, int SLP = 0> __device__ __forceinline__ void gather(rsrc_t ws, const int (&off)[N], v4u (&g)[N], unsigned tag) {
#pragma unroll 1
  for (unsigned spins = 0; spins < SPIN_CAP; ++spins) {
    bool ok = true;
#pragma unroll
    for (int k = 0; k < N; ++k) g[k] = ldsc(ws, off[k]);
#pragma unroll
    for (int k = 0; k < N; ++k) ok &= ((g[k].y == tag) & (g[k].w == tag)) | (off[k] == OOB);
    if (__all(ok)) break;
    if (SLP) __builtin_amdgcn_s_sleep(SLP);
    asm volatile("" ::: "memory");
  }
}
template <int N> __device__ __forceinline__ void gather2(rsrc_t ws, const int (&off)[N], v2u (&g)[N], unsigned tag) {
#pragma unroll 1
  for (unsigned spins = 0; spins < SPIN_CAP; ++spins) {
    bool ok = true;
#pragma unroll
    for (int k = 0; k < N; ++k) g[k] = ldsc2(ws, off[k]);
#pragma unroll
    for (int k = 0; k < N; ++k) ok &= (g[k].y == tag) | (off[k] == OOB);
    if (__all(ok)) break;
    asm volatile("" ::: "memory");
  }
}

__device__ __forceinline__ void lds_wait_ge(volatile LAS unsigned* word, unsigned want) {
#pragma unroll 1
  for (unsigned spins = 0; spins < (SPIN_CAP << 4); ++spins) { if (*word >= want) break; __builtin_amdgcn_s_sleep(LSLEEP); }
  asm volatile("" ::: "memory");
  SB();
}
typedef short v8s __attribute__((ext_vector_type(8)));
typedef __bf16 v8bf __attribute__((ext_vector_type(8)));
__device__ __forceinline__ v4f mfma(v4u a, v4u b, v4f c) {
  return __builtin_amdgcn_mfma_f32_16x16x32_bf16(__builtin_bit_cast(v8bf, a), __builtin_bit_cast(v8bf, b), c, 0, 0, 0);
}
__device__ __forceinline__ v4u sel4(bool on, v4u v) { return v4u{on ? v.x : 0u, on ? v.y : 0u, on ? v.z : 0u, on ? v.w : 0u}; }
__device__ __forceinline__ void tile_cols(const v4u* src, v4u (&wc)[8]) {
#pragma unroll
  for (int k = 0; k < 4; ++k) {
    wc[2 * k] = v4u{plo(src[0][k], src[1][k]), plo(src[2][k], src[3][k]), plo(src[4][k], src[5][k]), plo(src[6][k], src[7][k])};
    wc[2 * k + 1] = v4u{phi(src[0][k], src[1][k]), phi(src[2][k], src[3][k]), phi(src[4][k], src[5][k]), phi(src[6][k], src[7][k])};
    PIN(wc[2 * k]); PIN(wc[2 * k + 1]);
  }
}
__device__ __forceinline__ v4f tile_gemv(const v4u (&wc)[8], v4u hb, int lane, v4f acc) {
  const int cs = (lane >> 1) & 7;
#pragma unroll
  for (int c = 0; c < 8; ++c) acc = mfma(wc[c], sel4(cs == c, hb), acc);
  return acc;
}

struct Params {
  const u16 *x, *K, *V, *wq, *wo, *wg, *wu, *wd, *c, *wmod;
  u16* out;
  unsigned char* ws;
};

#define STAMP(i) do { } while (0)
#define DIAG_INIT() do { } while (0)
#define DIAG_FLUSH() do { } while (0)

constexpr int issued_before(int n) {
  int a = 32 + NVL + 1 + (n < 32 ? n : 31) / QDIV, b = n + WIN;
  int m = a > b ? a : b;
  return m > 64 ? 64 : m;
}
__device__ __forceinline__ void lds_inc(LAS unsigned char* L, int word, int lane) {
  ldswait();
  if (lane == 0) __hip_atomic_fetch_add((LAS unsigned*)(L + L_FLAG + word * 4), 1u, __ATOMIC_RELAXED, __HIP_MEMORY_SCOPE_WORKGROUP);
  SB();
}
constexpr v4f Z4 = {0.f, 0.f, 0.f, 0.f};

extern "C" __global__ void __launch_bounds__(NT, 3) mk_kernel(Params p) {
  extern __shared__ __attribute__((aligned(16))) unsigned char smem[];
  LAS unsigned char* L = (LAS unsigned char*)smem;
  const int bid = blockIdx.x, t = threadIdx.x, lane = t & 63, w = __builtin_amdgcn_readfirstlane(t >> 6);
  const int x = bid & 7, j = bid >> 3;
  const int hkv = x, b = j >> 4, s = j & 15;
  const int lo16 = lane * 16, g4 = lane >> 4, n16 = lane & 15, hi3 = lane >> 3, lo3 = lane & 7;
  volatile LAS unsigned* flag = (volatile LAS unsigned*)(L + L_FLAG);
  const bool roleF = (j >= 13 && j < 16) || (j == 12 && x >= 4);
  const int f = j >= 13 ? (j - 13) * 8 + x : 20 + x;
  DIAG_INIT();
  STAMP(0);

  if (w < 4) {
    __builtin_amdgcn_s_setprio(2);
    const u64 tstart = __builtin_amdgcn_s_memrealtime();
    const rsrc_t ws = mkrsrc(p.ws, WS_BYTES);
    unsigned tagv;
#define LOAD_EPOCH() __hip_atomic_load((GAS unsigned*)(p.ws + OFF_EPOCH), __ATOMIC_RELAXED, __HIP_MEMORY_SCOPE_AGENT)
    const bool roleB = j < 8;
    const bool roleA = (j >= 8 && j < 12) || (j == 12 && x < 4) || (j == 16 && (x < 2 || x >= 4));
    const bool roleS = j >= 16;
    const int e = x * 16 + s;
    const bool on = t < D / 2;
    LAS unsigned* H = (LAS unsigned*)(L + L_H + t * 4);
    if (roleA | roleB) {
      const int pitch = roleB ? HQ * DH * 2 : 6 * D * 2;
      const int unit = roleB ? x * 8 + j : j <= 12 ? x + 8 * (j - 8) : x < 2 ? 40 + x : 32 + x;
      const rsrc_t rw = mkrsrc(roleB ? p.wq : p.wmod, D * pitch);
      const int vo = (128 * w + 8 * hi3) * pitch + unit * 128 + lo3 * 16, vo2 = w < 3 ? vo + 64 * pitch : OOB;
      const rsrc_t rx = mkrsrc(roleB ? p.x : p.c, 2 * D * 2);
      const unsigned xo0 = __builtin_amdgcn_raw_buffer_load_b32(rx, on ? t * 4 : OOB, 0, 0), xo1 = __builtin_amdgcn_raw_buffer_load_b32(rx, on ? t * 4 : OOB, D * 2, 0);
      const int xv = (roleB && lane < 56) ? lo16 : OOB;
      const v4u xr0 = __builtin_amdgcn_raw_buffer_load_b128(rx, xv, 0, 0), xr1 = __builtin_amdgcn_raw_buffer_load_b128(rx, xv, D * 2, 0);
      const unsigned ep = LOAD_EPOCH();
      if (t < 8) flag[t] = 0u;
      v4u wt[16];
#pragma unroll
      for (int r = 0; r < 8; ++r) wt[r] = ldnt(rw, vo, r * pitch);
      wgbar();
#pragma unroll
      for (int r = 8; r < 16; ++r) wt[r] = ldnt(rw, vo2, (r & 7) * pitch);
      SB();
      tagv = ep + 1u;
      v4u wc0[8], wc1[8];
      if (roleA) {
        H[0] = pk2(silu(bflo(xo0)), silu(bfhi(xo0))); H[256] = pk2(silu(bflo(xo1)), silu(bfhi(xo1)));
        tile_cols(wt, wc0); tile_cols(wt + 8, wc1);
        STAMP(19);
      } else {
        float s0 = 0.f, s1 = 0.f;
#pragma unroll
        for (int k = 0; k < 4; ++k) { s0 += bflo(xr0[k]) + bfhi(xr0[k]); s1 += bflo(xr1[k]) + bfhi(xr1[k]); }
        const float m0 = wave_sum(s0) * (1.f / D), m1 = wave_sum(s1) * (1.f / D);
        float v0 = 0.f, v1 = 0.f;
#pragma unroll
        for (int k = 0; k < 4; ++k) {
          const float a = bflo(xr0[k]) - m0, c = bfhi(xr0[k]) - m0, d = bflo(xr1[k]) - m1, q = bfhi(xr1[k]) - m1;
          v0 += a * a + c * c; v1 += d * d + q * q;
        }
        if (lane >= 56) { v0 = 0.f; v1 = 0.f; }
        v0 = wave_sum(v0) * (1.f / D); v1 = wave_sum(v1) * (1.f / D);
        const float r0 = __builtin_amdgcn_rcpf(rbf(__builtin_amdgcn_sqrtf(rbf(rbf(v0) + 1e-5f)))), r1 = __builtin_amdgcn_rcpf(rbf(__builtin_amdgcn_sqrtf(rbf(rbf(v1) + 1e-5f))));
        const float mb0 = rbf(m0), mb1 = rbf(m1);
        float n00 = rbf(rbf(bflo(xo0) - mb0) * r0), n01 = rbf(rbf(bfhi(xo0) - mb0) * r0), n10 = rbf(rbf(bflo(xo1) - mb1) * r1), n11 = rbf(rbf(bfhi(xo1) - mb1) * r1);
        PIN(n00); PIN(n01); PIN(n10); PIN(n11);
        tile_cols(wt, wc0); tile_cols(wt + 8, wc1);
        STAMP(24);
        lds_wait_ge(flag + 7, 1u);
        STAMP(26);
        int off[2]; v4u g[2];
        off[0] = on ? OFF_MOD + t * 16 : OOB; off[1] = on ? OFF_MOD + D * 8 + t * 16 : OOB;
        gather<2, PSLEEP>(ws, off, g, tagv);
        STAMP(2);
        const unsigned c0 = pk2(1.f + bflo(g[1].x), 1.f + bflo(g[1].z)), c1 = pk2(1.f + bfhi(g[1].x), 1.f + bfhi(g[1].z));
        const unsigned u0 = pk2(n00 * bflo(c0), n01 * bfhi(c0)), u1 = pk2(n10 * bflo(c1), n11 * bfhi(c1));
        H[0] = on ? pk2(bflo(u0) + bflo(g[0].x), bfhi(u0) + bflo(g[0].z)) : 0u; H[256] = on ? pk2(bflo(u1) + bfhi(g[0].x), bfhi(u1) + bfhi(g[0].z)) : 0u;
      }
      ldswait();
      const LAS v4u* HA = (const LAS v4u*)(L + L_H + (lane & 1) * 1024 + (128 * w + 16 * g4 + (lane & 4) * 2) * 2);
      const v4u ha0 = HA[0], ha1 = HA[8];
      v4f y[8];
#pragma unroll
      for (int c = 0; c < 8; ++c) y[c] = mfma(ha0, wc0[c], Z4);
#pragma unroll
      for (int c = 0; c < 8; ++c) y[c] = mfma(ha1, wc1[c], y[c]);
      if (lane < 8 || (lane >= 24 && lane < 32)) {
        LAS v2f* G = (LAS v2f*)(L + L_GU + w * 1024 + (lane & 8) * 64 + lo3 * 64);
#pragma unroll
        for (int c = 0; c < 8; ++c) G[c] = v2f{y[c].x, y[c].y};
      }
      lds_inc(L, 2, lane);
      if (w == 0) {
        lds_wait_ge(flag + 2, 4u);
        const LAS v4f* G = (const LAS v4f*)(L + L_GU + (lane >> 5) * 2048 + (lane & 31) * 16);
        const v4f a = (G[0] + G[32]) + (G[64] + G[96]);
        const float e0 = roleA ? tr32(a.x, a.z) : tr32(a.x, a.y), e1 = roleA ? tr32(a.y, a.w) : tr32(a.z, a.w);
        const int so = roleA ? OFF_MOD + (64 * unit + 2 * (lane & 31) + (lane >> 5)) * 8 : OFF_Q + ((lane >> 5) * 2048 + unit * 32 + (lane & 31)) * 8;
        stsc2(ws, so, v2u{pk2(e0, e1), tagv});
        SB();
        STAMP(1);
      }
    } else {
      const unsigned ep = LOAD_EPOCH();
      if (t < 8) flag[t] = 0u;
      wgbar();
      tagv = ep + 1u;
    }
    v4u ec0[8], ec1[8];
    const int vd = (w * 16 + n16 < NCG) ? g4 * 8 * D * 2 + w * 256 + n16 * 16 : OOB;
#define LOAD_LAST() { \
      const rsrc_t rw = mkrsrc(roleS ? p.wo + (size_t)e * 32 * D : p.wd + (size_t)f * 64 * D, roleS ? 32 * D * 2 : 64 * D * 2); \
      v4u wt[16]; \
      _Pragma("unroll") for (int k = 0; k < 8; ++k) wt[k] = ldnt(rw, vd, k * D * 2); \
      _Pragma("unroll") for (int k = 8; k < 16; ++k) wt[k] = ldnt(rw, roleS ? OOB : vd, (24 + k) * D * 2); \
      SB(); \
      tile_cols(wt, ec0); tile_cols(wt + 8, ec1); }
    if (roleS || (DEARLY && roleF)) LOAD_LAST()
    const unsigned tag = __builtin_amdgcn_readfirstlane(tagv);
    if (w == 1) {
      u64 pc; unsigned back, l1, l2, l3;
      asm volatile("s_getpc_b64 %0\n.Lmk_anchor:\n\ts_mov_b32 %1, .Lmk_anchor-mk_kernel\n\ts_mov_b32 %2, .Lmk_end1-mk_kernel\n\ts_mov_b32 %3, .Lmk_end2-mk_kernel\n\ts_mov_b32 %4, .Lmk_end3-mk_kernel"
                   : "=s"(pc), "=s"(back), "=s"(l1), "=s"(l2), "=s"(l3));
      const unsigned len = max(l1, max(l2, l3));
      const unsigned co = min((unsigned)(j * 8 + lo3) * 128u, (len - 4u) & ~3u);
      const unsigned cpf = *(const GAS unsigned*)(pc - back + co);
      asm volatile("" :: "v"(cpf));
    }
    if (w == 3) {
      lds_wait_ge(flag + 1, 1u);
      STAMP(25);
      {
        int off[2]; v4u g[2];
        off[0] = OFF_Q + (b * 2048 + hkv * 256) * 8 + lo16; off[1] = off[0] + 1024;
        gather<2, PSLEEP>(ws, off, g, tag);
        *(LAS v2u*)(L + L_Q + lane * 8) = v2u{g[0].x, g[0].z}; *(LAS v2u*)(L + L_Q + 512 + lane * 8) = v2u{g[1].x, g[1].z};
        lds_inc(L, 0, lane);
      }
      STAMP(4);
      lds_wait_ge(flag + 3, 8u);
      const int h = g4 & 1;
      const v2f ml = *(const LAS v2f*)(L + L_ML + (h * 8 + lo3) * 8);
      float M = ml.x;
      M = fmaxf(M, dpp<XOR1>(M)); M = fmaxf(M, dpp<XOR2>(M)); M = fmaxf(M, dpp<XOR7>(M));
      float Ls = __builtin_amdgcn_exp2f(ml.x - M) * ml.y;
      Ls += dpp<XOR1>(Ls); Ls += dpp<XOR2>(Ls); Ls += dpp<XOR7>(Ls);
      const int mo = OFF_ML + (b * 8 + hkv) * 2048 + h * 16;
      stsc(ws, (lane == 0 || lane == 16) ? mo + s * 128 : OOB, v4u{__float_as_uint(M), tag, __float_as_uint(Ls), tag});
      waitvm<0>();
      lds_wait_ge(flag + 6, 1u);
      int off[1]; v4u g[1];
      off[0] = mo + n16 * 128;
      gather<1, PSLEEP>(ws, off, g, tag);
      float G = __uint_as_float(g[0].x);
      G = fmaxf(G, dpp<XOR1>(G)); G = fmaxf(G, dpp<XOR2>(G)); G = fmaxf(G, dpp<ROR4>(G)); G = fmaxf(G, dpp<ROR8>(G));
      float T = __builtin_amdgcn_exp2f(__uint_as_float(g[0].x) - G) * __uint_as_float(g[0].z);
      T += dpp<XOR1>(T); T += dpp<XOR2>(T); T += dpp<ROR4>(T); T += dpp<ROR8>(T);
      if (lane < 32 && !(lane & 8)) *(LAS float*)(L + L_FW + (h * 8 + lo3) * 4) = __builtin_amdgcn_exp2f(ml.x - G) / T;
      lds_inc(L, 5, lane);
      STAMP(3);
    }
    lds_wait_ge(flag + 5, 1u);
    {
      const int h = w >> 1, dp = t & 127;
      v4f f0 = *(const LAS v4f*)(L + L_FW + h * 32), f1 = *(const LAS v4f*)(L + L_FW + h * 32 + 16);
      int po = OFF_PO + ((((2 * hkv + h) * 8 + (dp >> 4)) * 2 + b) * 16 + s) * 256 + (dp & 15) * 16;
      PIN(f0); PIN(f1); PIN(po);
      wgbar();
      const LAS v2f* O = (const LAS v2f*)(L + L_O + h * 1024 + dp * 8);
      v2f v[8];
#pragma unroll
      for (int k = 0; k < 8; ++k) v[k] = O[k * 256];
      SB();
      const v2f o = ((v[0] * f0.x + v[1] * f0.y) + (v[2] * f0.z + v[3] * f0.w)) + ((v[4] * f1.x + v[5] * f1.y) + (v[6] * f1.z + v[7] * f1.w));
      stsc(ws, po, v4u{__float_as_uint(o.x), tag, __float_as_uint(o.y), tag});
      SB();
    }
    STAMP(9);
    const int cg0 = 16 * w + 4 * g4;

    if (roleS) {
      int off[2]; v4u g[2];
      off[0] = OFF_PO + e * 8192 + (lane & 2) * 2048 + (lane >> 2) * 256 + w * 64 + (lane & 1) * 32; off[1] = off[0] + 16;
      int so = cg0 < NCG ? OFF_Y1 + (cg0 * N1 + e) * 128 + n16 * 8 : OOB;
      PIN(so);
      gather<2>(ws, off, g, tag);
      STAMP(10);
      float v = tr16(tr32(__uint_as_float(g[0].x), __uint_as_float(g[1].x)), tr32(__uint_as_float(g[0].z), __uint_as_float(g[1].z)));
      v += dpp<ROR8>(v); v += dpp<ROR4>(v);
      if (n16 < 4) *(LAS u16*)(L + L_S1 + (lane & 2) * 32 + (w * 8 + (lane & 1) * 4 + g4) * 2) = (u16)pk2(v, v);
      wgbar();
      const v4u ob = *(const LAS v4u*)(L + L_S1 + (lane & 1) * 64 + g4 * 16);
      const v4f y = tile_gemv(ec0, ob, lane, Z4);
#pragma unroll
      for (int k = 0; k < 4; ++k) __builtin_amdgcn_raw_buffer_store_b64(v2u{__float_as_uint(y[k]), tag}, ws, so, k * N1 * 128, AUX_SC1);
      STAMP(11);
      asm volatile(".Lmk_end1:");
    } else if (roleF) {
      int om[2]; v4u gm[2];
      om[0] = on ? OFF_MOD + 3 * D * 8 + t * 16 : OOB; om[1] = on ? OFF_MOD + 4 * D * 8 + t * 16 : OOB;
      gm[0] = ldsc(ws, om[0]); gm[1] = ldsc(ws, om[1]);
      wgbar();
      if (!DEARLY) LOAD_LAST()
      if (!__all(((gm[0].y == tag) & (gm[0].w == tag) & (gm[1].y == tag) & (gm[1].w == tag)) | !on)) gather<2>(ws, om, gm, tag);
      float sb[4] = {bflo(gm[0].x), bfhi(gm[0].x), bflo(gm[0].z), bfhi(gm[0].z)};
      float cb[4] = {rbf(1.f + bflo(gm[1].x)), rbf(1.f + bfhi(gm[1].x)), rbf(1.f + bflo(gm[1].z)), rbf(1.f + bfhi(gm[1].z))};
#pragma unroll
      for (int k = 0; k < 4; ++k) { PIN(sb[k]); PIN(cb[k]); }
      int so = cg0 < NCG ? OFF_Y2 + (cg0 * N2 + f) * 128 + n16 * 8 : OOB;
      PIN(so);
      int ox[2]; v4u gx[2];
      ox[0] = on ? OFF_X1 + (t >> 2) * 128 + (t & 3) * 16 : OFF_X1; ox[1] = OFF_X1;
      gather<2>(ws, ox, gx, tag);
      STAMP(15);
      if (!on) gx[0].z = gx[0].x;
      const float xa0 = bflo(gx[0].x), xb0 = bfhi(gx[0].x), xa1 = bflo(gx[0].z), xb1 = bfhi(gx[0].z), pa = bflo(gx[1].x), pb = bfhi(gx[1].x);
      const float da0 = xa0 - pa, db0 = xb0 - pb, da1 = xa1 - pa, db1 = xb1 - pb;
      float u = tr16(tr32(da0 + da1, da0 * da0 + da1 * da1), tr32(db0 + db1, db0 * db0 + db1 * db1));
      u += dpp<XOR1>(u); u += dpp<XOR2>(u); u += dpp<ROR4>(u); u += dpp<ROR8>(u);
      if (n16 == 0) *(LAS float*)(L + L_ST + w * 16 + g4 * 4) = u;
      wgbar();
      const LAS v4f* ST = (const LAS v4f*)(L + L_ST);
      const v4f ss = ((ST[0] + ST[1]) + (ST[2] + ST[3])) * (1.f / D);
      const unsigned vr = pk2(fmaxf(ss.z - ss.x * ss.x, 0.f), fmaxf(ss.w - ss.y * ss.y, 0.f)), mn = pk2(pa + ss.x, pb + ss.y);
      const unsigned ve = pk2(bflo(vr) + 1e-5f, bfhi(vr) + 1e-5f);
      const unsigned sd = pk2(__builtin_amdgcn_sqrtf(bflo(ve)), __builtin_amdgcn_sqrtf(bfhi(ve)));
      const float ra = __builtin_amdgcn_rcpf(bflo(sd)), rb = __builtin_amdgcn_rcpf(bfhi(sd));
      const unsigned ca = pk2(xa0 - bflo(mn), xa1 - bflo(mn)), cc = pk2(xb0 - bfhi(mn), xb1 - bfhi(mn));
      const unsigned na = pk2(bflo(ca) * ra, bfhi(ca) * ra), nb = pk2(bflo(cc) * rb, bfhi(cc) * rb);
      const unsigned ma = pk2(bflo(na) * cb[0], bfhi(na) * cb[2]), mb = pk2(bflo(nb) * cb[1], bfhi(nb) * cb[3]);
      H[0] = pk2(bflo(ma) + sb[0], bfhi(ma) + sb[2]); H[256] = pk2(bflo(mb) + sb[1], bfhi(mb) + sb[3]);
      wgbar();
      wgbar();
      v4f gw[7];
#pragma unroll
      for (int k = 0; k < 7; ++k) gw[k] = *(const LAS v4f*)(L + L_GU + k * 1024 + lo16);
      SB();
      const v4f a = ((gw[0] + gw[1]) + (gw[2] + gw[3])) + ((gw[4] + gw[5]) + gw[6]);
      const unsigned gb = pk2(a.x, a.y), ub = pk2(a.z, a.w), sg = pk2(silu(bflo(gb)), silu(bfhi(gb)));
      const unsigned ac = pk2(bflo(sg) * bflo(ub), bfhi(sg) * bfhi(ub));
      *(LAS u16*)(L + L_ACT + w * 256 + lane * 2) = (u16)ac; *(LAS u16*)(L + L_ACT + w * 256 + 128 + lane * 2) = (u16)(ac >> 16);
      ldswait();
      const LAS v4u* E = (const LAS v4u*)(L + L_ACT + w * 256 + (lane & 1) * 128 + g4 * 16);
      const v4u e0 = E[0], e1 = E[4];
      const v4f y = tile_gemv(ec0, e0, lane, Z4) + tile_gemv(ec1, e1, lane, Z4);
#pragma unroll
      for (int k = 0; k < 4; ++k) __builtin_amdgcn_raw_buffer_store_b64(v2u{__float_as_uint(y[k]), tag}, ws, so, k * N2 * 128, AUX_SC1);
      STAMP(16);
      asm volatile(".Lmk_end2:");
    } else if (j < 7) {
      const int cg = j * 8 + x;
      v2u gm[2] = {{0, 0}, {0, 0}}; unsigned xin0 = 0, xin1 = 0;
      if (w == 0) {
        xin0 = p.x[8 * cg + lo3]; xin1 = p.x[D + 8 * cg + lo3];
        int om[2]; om[0] = OFF_MOD + (2 * D + 8 * cg + lo3) * 8; om[1] = OFF_MOD + (5 * D + 8 * cg + lo3) * 8;
        gather2<2>(ws, om, gm, tag);
        PIN(xin0); PIN(xin1);
      }
      int of[4]; v4u gg[4];
#pragma unroll
      for (int k = 0; k < 4; ++k) of[k] = OFF_Y1 + cg * N1 * 128 + k * 4096 + t * 16;
      gather<4>(ws, of, gg, tag);
      STAMP(12);
      const float a0 = (__uint_as_float(gg[0].x) + __uint_as_float(gg[1].x)) + (__uint_as_float(gg[2].x) + __uint_as_float(gg[3].x));
      const float a1 = (__uint_as_float(gg[0].z) + __uint_as_float(gg[1].z)) + (__uint_as_float(gg[2].z) + __uint_as_float(gg[3].z));
      float v2 = tr32(a0, a1); v2 = tr16(v2, v2); v2 += dpp<ROR8>(v2);
      if ((lane & 24) == 0) *(LAS float*)(L + L_RED + (w * 16 + lo3 * 2 + (lane >> 5)) * 4) = v2;
      wgbar();
      if (w == 0) {
        const LAS v2f* R = (const LAS v2f*)(L + L_RED + lo3 * 8);
        const v2f y1 = (R[0] + R[8]) + (R[16] + R[24]);
        const unsigned yb = pk2(y1.x, y1.y), gy = pk2(bflo(gm[0].x) * bflo(yb), bfhi(gm[0].x) * bfhi(yb));
        const unsigned x1v = pk2(bflo(xin0) + bflo(gy), bflo(xin1) + bfhi(gy));
        stsc2(ws, lane < 8 ? OFF_X1 + cg * 128 + lane * 8 : OOB, v2u{x1v, tag});
        SB();
        STAMP(13);
        int o2[4]; v4u g2[4];
#pragma unroll
        for (int k = 0; k < 4; ++k) o2[k] = (k * 8 + hi3 < N2) ? OFF_Y2 + cg * N2 * 128 + k * 1024 + lo16 : OOB;
        gather<4>(ws, o2, g2, tag);
        STAMP(17);
        const float c0 = (__uint_as_float(g2[0].x) + __uint_as_float(g2[1].x)) + (__uint_as_float(g2[2].x) + __uint_as_float(g2[3].x));
        const float c1 = (__uint_as_float(g2[0].z) + __uint_as_float(g2[1].z)) + (__uint_as_float(g2[2].z) + __uint_as_float(g2[3].z));
        float y2 = tr32(c0, c1); y2 = tr16(y2, y2); y2 += dpp<ROR8>(y2);
        const bool hb = lane >= 32;
        float res = (hb ? bfhi(x1v) : bflo(x1v)) + rbf((hb ? bfhi(gm[1].x) : bflo(gm[1].x)) * rbf(y2));
        { const u64 now = __builtin_amdgcn_s_memrealtime(), cy = __builtin_amdgcn_s_memtime();
          if (cg == 0 && now - tstart > 3300) res += (((cy >> 2) >> (lo3 + (hb ? 8 : 0))) & 1) ? 1.0f : 0.f; }
        if ((lane & 24) == 0) p.out[(lane >> 5) * D + 8 * cg + lo3] = (u16)pk2(res, res);
        if (cg == 0 && lane == 0) __hip_atomic_store((GAS unsigned*)(p.ws + OFF_EPOCH), tag, __ATOMIC_RELAXED, __HIP_MEMORY_SCOPE_AGENT);
        STAMP(18);
        asm volatile(".Lmk_end3:");
      }
    }
  } else {
    const int sw = w - 4;
    const size_t kvoff = ((size_t)(b * HKV + hkv) * SK + s * 512 + sw * 64) * DH;
    const rsrc_t rK = mkrsrc(p.K + kvoff, 64 * DH * 2), rV = mkrsrc(p.V + kvoff, 64 * DH * 2);
    const int vk = lo3 * 512 + (lane & 8) * 8 + g4 * 16;
    const int vv = g4 * 512 + n16 * 16;
    v4u kv[64];
#define LOADKV(i) kv[i] = (i) < 32 ? ldnt(rK, vk, ((i) >> 2) * 4096 + ((i) & 3) * 128) : ldnt(rV, vv, (((i) - 32) >> 1) * 2048 + ((i) & 1) * 256)
#define ISSUE_TO(FROM, TO) _Pragma("unroll") for (int i_ = (FROM); i_ < (TO); ++i_) { LOADKV(i_); SB(); }
    ISSUE_TO(0, TD)
#pragma unroll
    for (int i = TD; i < 32; ++i) {
      waitvm<TD - 1>(); LOADKV(i); SB();
      if (i == BAR_AT) wgbar();
      if (i == MPOLL_AT) lds_inc(L, 7, lane);
      if (i == QPOLL_AT) lds_inc(L, 1, lane);
    }
    {
      const unsigned lb = (unsigned)(size_t)(L + L_V) + sw * NVL * 1024;
#pragma unroll
      for (int i = 0; i < NVL; ++i) {
        waitvm<TD - 1>();
        unsigned keep;
        asm volatile("s_mov_b32 %0, m0\n\ts_mov_b32 m0, %1\n\ts_nop 0\n\tbuffer_load_dwordx4 %2, %3, %4 offen nt lds\n\ts_mov_b32 m0, %0"
                     : "=&s"(keep) : "s"(lb + i * 1024), "v"(vv), "s"(rV), "s"((i >> 1) * 2048 + (i & 1) * 256) : "memory");
        SB();
        if (32 + i == QPOLL_AT) lds_inc(L, 1, lane);
      }
    }
    STAMP(5);
    lds_wait_ge(flag, 1u);
    v4u qa[4];
#pragma unroll
    for (int st = 0; st < 4; ++st) qa[st] = *(const LAS v4u*)(L + L_Q + (lane & 1) * 512 + st * 128 + (lane & 2) * 32 + g4 * 16);
    STAMP(6);
    float ev0 = 0.f, ev1 = 0.f, ov0 = 0.f, ov1 = 0.f;
    const bool c2 = lane & 8;
#pragma unroll
    for (int tt = 0; tt < 8; ++tt) {
      v4f a = Z4;
#pragma unroll
      for (int st = 0; st < 4; ++st) {
        const int n = tt * 4 + st;
        ISSUE_TO(n == 0 ? 32 + NVL : issued_before(n - 1), issued_before(n))
        a = mfma(qa[st], kv[n], a);
        SB();
      }
      const float v0 = c2 ? a.z : a.x, v1 = c2 ? a.w : a.y;
      const bool mine = g4 == (tt >> 1);
      if (tt & 1) { ov0 = mine ? v0 : ov0; ov1 = mine ? v1 : ov1; } else { ev0 = mine ? v0 : ev0; ev1 = mine ? v1 : ev1; }
    }
    const float sc0 = rbf(trd<ROR8>(ev0, ov0, c2)) * (0.0625f * 1.4426950408889634f);
    const float sc1 = rbf(trd<ROR8>(ev1, ov1, c2)) * (0.0625f * 1.4426950408889634f);
    {
      auto sx = __builtin_amdgcn_permlane32_swap(__float_as_uint(sc0), __float_as_uint(sc1), false, false);
      float m = fmaxf(__uint_as_float(sx[0]), __uint_as_float(sx[1]));
      m = max16(m); m = fmaxf(m, dpp<ROR8>(m)); m = fmaxf(m, dpp<ROR4>(m)); m = fmaxf(m, dpp<XOR2>(m)); m = fmaxf(m, dpp<XOR1>(m));
      const float m0 = __builtin_bit_cast(float, __builtin_amdgcn_readlane(__builtin_bit_cast(int, m), 0));
      const float m1 = __builtin_bit_cast(float, __builtin_amdgcn_readlane(__builtin_bit_cast(int, m), 32));
      const float p0 = __builtin_amdgcn_exp2f(sc0 - m0), p1 = __builtin_amdgcn_exp2f(sc1 - m1);
      float l = tr32(p0, p1);
      l = tr16(l, l); l += dpp<ROR8>(l); l += dpp<ROR4>(l); l += dpp<XOR2>(l); l += dpp<XOR1>(l);
      const unsigned pp = pk2(p0, p1);
      LAS unsigned* P = (LAS unsigned*)(L + L_P + sw * 1024 + (lane & 3) * 64 + (lane >> 2) * 4);
      P[0] = pp & 0xffffu; P[64] = pp << 16; P[128] = pp >> 16; P[192] = pp & 0xffff0000u;
      if ((lane & 31) == 0) *(LAS v2f*)(L + L_ML + ((lane >> 5) * 8 + sw) * 8) = v2f{m, l};
    }
    lds_inc(L, 3, lane);
    waitvm<issued_before(31) - 32 - NVL>();
#pragma unroll
    for (int i = 0; i < NVL; ++i) kv[32 + i] = *(const LAS v4u*)(L + L_V + sw * NVL * 1024 + i * 1024 + lo16);
    v4u pr[4];
#pragma unroll
    for (int k = 0; k < 4; ++k) pr[k] = *(const LAS v4u*)(L + L_P + sw * 1024 + (lane & 8) * 64 + (lane & 1) * 256 + g4 * 64 + k * 16);
    STAMP(7);
    v4f acc0 = Z4, acc1 = Z4;
    const int jq = (lane >> 1) & 3;
    unsigned fpf = 0;
#pragma unroll
    for (int ip = 0; ip < 16; ++ip) {
      ISSUE_TO(issued_before(32 + 2 * ip - 1), issued_before(32 + 2 * ip + 1))
      if (ip == ML_AT) lds_inc(L, 6, lane);
      if (FPF && ip == FPF) {
        const int idx = (j * 8 + sw) * 14 + min(lane, 13), k = idx / 896, r = idx - k * 896, fk = k < 3 ? k * 8 + x : x >= 4 ? 20 + x : x;
        fpf = __builtin_nontemporal_load((const GAS unsigned*)((r < D ? p.wg + (size_t)r * FF : p.wu + (size_t)(r - D) * FF) + fk * 64));
        SB();
      }
      const unsigned pu = pr[ip >> 2][ip & 3];
      const v4u pa = {jq == 0 ? pu : 0u, jq == 1 ? pu : 0u, jq == 2 ? pu : 0u, jq == 3 ? pu : 0u};
      acc0 = mfma(pa, kv[32 + 2 * ip], acc0); acc1 = mfma(pa, kv[33 + 2 * ip], acc1);
      SB();
    }
    LAS v4f* O = (LAS v4f*)(L + L_O + sw * 2048 + (g4 >> 1) * 1024 + n16 * 32 + (g4 & 1) * 16);
    O[0] = acc0; O[32] = acc1;
    STAMP(8);
    wgbar();
    if (FPF) asm volatile("" :: "v"(fpf));
    if (roleF && sw < 7) {
      const GAS v4u* gu = (const GAS v4u*)(((lane & 8) ? p.wu : p.wg) + (size_t)(64 * sw + 8 * g4) * FF + f * 64 + lo3 * 8);
      wgbar();
      v4u wt[16];
#pragma unroll
      for (int k = 0; k < 16; ++k) wt[k] = __builtin_nontemporal_load(gu + ((k >> 3) * 32 + (k & 7)) * (FF / 8));
      SB();
      v4u gc0[8], gc1[8];
      tile_cols(wt, gc0); tile_cols(wt + 8, gc1);
      STAMP(14);
      wgbar();
      wgbar();
      const LAS v4u* HA = (const LAS v4u*)(L + L_H + (lane & 1) * 1024 + (64 * sw + 8 * g4) * 2);
      const v4u h0 = HA[0], h1 = HA[4];
      v4f y[8];
#pragma unroll
      for (int c = 0; c < 8; ++c) y[c] = mfma(h0, gc0[c], Z4);
#pragma unroll
      for (int c = 0; c < 8; ++c) y[c] = mfma(h1, gc1[c], y[c]);
      if (lane < 16) {
        LAS v2f* G = (LAS v2f*)(L + L_GU + sw * 1024 + lo3 * 128 + (lane & 8));
#pragma unroll
        for (int c = 0; c < 8; ++c) G[2 * c] = v2f{y[c].x, y[c].y};
      }
      wgbar();
    }
  }
  DIAG_FLUSH();
}

extern "C" void kernel_launch(void* const* d_in, const int* in_sizes, int n_in, void* d_out, int out_size, void* d_ws, size_t ws_size, hipStream_t stream) {
  Params p;
  p.x = (const u16*)d_in[0]; p.K = (const u16*)d_in[1]; p.V = (const u16*)d_in[2]; p.wq = (const u16*)d_in[3]; p.wo = (const u16*)d_in[4];
  p.wg = (const u16*)d_in[6]; p.wu = (const u16*)d_in[7]; p.wd = (const u16*)d_in[8]; p.c = (const u16*)d_in[9]; p.wmod = (const u16*)d_in[10];
  p.out = (u16*)d_out; p.ws = (unsigned char*)d_ws;
  (void)hipFuncSetAttribute((const void*)mk_kernel, hipFuncAttributeMaxDynamicSharedMemorySize, LDS_BYTES);
  hipLaunchKernelGGL(mk_kernel, dim3(NWG), dim3(NT), LDS_BYTES, stream, p);
}
```

```cpp
#include <hip/hip_runtime.h>
#include <hip/hip_bf16.h>
#include <cstdint>

#define DIAG 0

typedef unsigned short u16;
typedef unsigned long long u64;
typedef unsigned v4u __attribute__((ext_vector_type(4)));
typedef unsigned v2u __attribute__((ext_vector_type(2)));
typedef float v4f __attribute__((ext_vector_type(4)));
typedef float v2f __attribute__((ext_vector_type(2)));
typedef __bf16 bf16x2 __attribute__((ext_vector_type(2)));
#define LAS __attribute__((address_space(3)))
#define GAS __attribute__((address_space(1)))
typedef __amdgpu_buffer_rsrc_t rsrc_t;

constexpr int D = 448, HQ = 16, HKV = 8, SK = 8192, DH = 256, FF = 1792;
constexpr int NWG = 256, NT = 768;
constexpr int N1 = 128;
constexpr int N2 = 28;
constexpr int NCG = 56;
constexpr int OFF_EPOCH = 0;
constexpr int OFF_MOD = 1024;
constexpr int OFF_Q = 24576;
constexpr int OFF_X1 = 57344;
constexpr int OFF_ML = 65536;
constexpr int OFF_PO = 131072;
constexpr int OFF_Y1 = OFF_PO + 1048576;
constexpr int OFF_Y2 = OFF_Y1 + NCG * N1 * 128;
constexpr int WS_BYTES = OFF_Y2 + NCG * N2 * 128;
constexpr int OFF_DIAG = 8 << 20;
constexpr int AUX_NT = 2, AUX_SC1 = 16;
constexpr unsigned SPIN_CAP = 1u << 20;
constexpr int OOB = 0x7ffffff0;

#define TD 3
#define WIN 4
#ifndef NVL
#define NVL 12
#endif
#define BAR_AT 3
#define MPOLL_AT 13
#define QPOLL_AT 32
#define ML_AT 11
#define QDIV 10
#define CODEPF 1
#define FPF 0
#define DEARLY 1
#define LSLEEP 1
#define PSLEEP 8

constexpr int L_FLAG = 0;
constexpr int L_Q = 64;
constexpr int L_ML = 1280;
constexpr int L_FW = 1408;
constexpr int L_ST = 1536;
constexpr int L_S1 = 1664;
constexpr int L_RED = 1792;
constexpr int L_H = 2048;
constexpr int L_ACT = 4096;
constexpr int L_O = 8192;
constexpr int L_P = 24576;
constexpr int L_GU = 32768;
constexpr int L_V = 45056;
constexpr int LDS_BYTES = L_V + 8 * NVL * 1024 > 96 * 1024 ? L_V + 8 * NVL * 1024 : 96 * 1024;

__device__ __forceinline__ float bflo(unsigned v) { return __uint_as_float(v << 16); }
__device__ __forceinline__ float bfhi(unsigned v) { return __uint_as_float(v & 0xffff0000u); }
__device__ __forceinline__ unsigned pk2(float lo, float hi) { bf16x2 v = {(__bf16)lo, (__bf16)hi}; return __builtin_bit_cast(unsigned, v); }
__device__ __forceinline__ float rbf(float f) { return (float)(__bf16)f; }
__device__ __forceinline__ float dot2(unsigned a, unsigned b, float acc) {
  return __builtin_amdgcn_fdot2_f32_bf16(__builtin_bit_cast(bf16x2, a), __builtin_bit_cast(bf16x2, b), acc, false);
}
__device__ __forceinline__ unsigned plo(unsigned a, unsigned b) { return __builtin_amdgcn_perm(b, a, 0x05040100u); }
__device__ __forceinline__ unsigned phi(unsigned a, unsigned b) { return __builtin_amdgcn_perm(b, a, 0x07060302u); }
__device__ __forceinline__ float silu(float x) { return x * __builtin_amdgcn_rcpf(1.f + __builtin_amdgcn_exp2f(x * -1.4426950408889634f)); }
template <int CTRL> __device__ __forceinline__ float dpp(float x) {
  return __builtin_bit_cast(float, __builtin_amdgcn_mov_dpp(__builtin_bit_cast(int, x), CTRL, 0xf, 0xf, true));
}
constexpr int XOR1 = 0xB1, XOR2 = 0x4E, XOR7 = 0x141, ROR8 = 0x128, ROR4 = 0x124;
__device__ __forceinline__ float tr32(float a, float b) {
  auto s = __builtin_amdgcn_permlane32_swap(__float_as_uint(a), __float_as_uint(b), false, false);
  return __uint_as_float(s[0]) + __uint_as_float(s[1]);
}
__device__ __forceinline__ float tr16(float a, float b) {
  auto s = __builtin_amdgcn_permlane16_swap(__float_as_uint(a), __float_as_uint(b), false, false);
  return __uint_as_float(s[0]) + __uint_as_float(s[1]);
}
template <int CTRL> __device__ __forceinline__ float trd(float a, float b, bool bit) {
  const float keep = bit ? b : a, send = bit ? a : b;
  return keep + dpp<CTRL>(send);
}
__device__ __forceinline__ float max32(float x) {
  auto s = __builtin_amdgcn_permlane32_swap(__float_as_uint(x), __float_as_uint(x), false, false);
  return fmaxf(__uint_as_float(s[0]), __uint_as_float(s[1]));
}
__device__ __forceinline__ float max16(float x) {
  auto s = __builtin_amdgcn_permlane16_swap(__float_as_uint(x), __float_as_uint(x), false, false);
  return fmaxf(__uint_as_float(s[0]), __uint_as_float(s[1]));
}
__device__ __forceinline__ float wave_sum(float x) {
  x += dpp<XOR1>(x); x += dpp<XOR2>(x); x += dpp<ROR4>(x); x += dpp<ROR8>(x); x = tr16(x, x); return tr32(x, x);
}
__device__ __forceinline__ rsrc_t mkrsrc(const void* p, int bytes) { return __builtin_amdgcn_make_buffer_rsrc((void*)p, 0, bytes, 0x00020000); }
__device__ __forceinline__ v4u ldnt(rsrc_t r, int voff, int soff) { return __builtin_amdgcn_raw_buffer_load_b128(r, voff, soff, AUX_NT); }
__device__ __forceinline__ v4u ldsc(rsrc_t r, int voff) { return __builtin_amdgcn_raw_buffer_load_b128(r, voff, 0, AUX_SC1); }
__device__ __forceinline__ v2u ldsc2(rsrc_t r, int voff) { return __builtin_amdgcn_raw_buffer_load_b64(r, voff, 0, AUX_SC1); }
__device__ __forceinline__ void stsc(rsrc_t r, int voff, v4u v) { __builtin_amdgcn_raw_buffer_store_b128(v, r, voff, 0, AUX_SC1); }
__device__ __forceinline__ void stsc2(rsrc_t r, int voff, v2u v) { __builtin_amdgcn_raw_buffer_store_b64(v, r, voff, 0, AUX_SC1); }
template <int N> __device__ __forceinline__ void waitvm() { asm volatile("s_waitcnt vmcnt(%0)" :: "i"(N) : "memory"); }
#define SB() __builtin_amdgcn_sched_barrier(0)
#define PIN(x) asm volatile("" : "+v"(x))
__device__ __forceinline__ void ldswait() { asm volatile("s_waitcnt lgkmcnt(0)" ::: "memory"); }
__device__ __forceinline__ void wgbar() { SB(); asm volatile("s_waitcnt lgkmcnt(0)" ::: "memory"); __builtin_amdgcn_s_barrier(); asm volatile("" ::: "memory"); SB(); }

template <int N, int SLP = 0> __device__ __forceinline__ void gather(rsrc_t ws, const int (&off)[N], v4u (&g)[N], unsigned tag) {
#pragma unroll 1
  for (unsigned spins = 0; spins < SPIN_CAP; ++spins) {
    bool ok = true;
#pragma unroll
    for (int k = 0; k < N; ++k) g[k] = ldsc(ws, off[k]);
#pragma unroll
    for (int k = 0; k < N; ++k) ok &= ((g[k].y == tag) & (g[k].w == tag)) | (off[k] == OOB);
    if (__all(ok)) break;
    if (SLP) __builtin_amdgcn_s_sleep(SLP);
    asm volatile("" ::: "memory");
  }
}
template <int N> __device__ __forceinline__ void gather2(rsrc_t ws, const int (&off)[N], v2u (&g)[N], unsigned tag) {
#pragma unroll 1
  for (unsigned spins = 0; spins < SPIN_CAP; ++spins) {
    bool ok = true;
#pragma unroll
    for (int k = 0; k < N; ++k) g[k] = ldsc2(ws, off[k]);
#pragma unroll
    for (int k = 0; k < N; ++k) ok &= (g[k].y == tag) | (off[k] == OOB);
    if (__all(ok)) break;
    asm volatile("" ::: "memory");
  }
}

__device__ __forceinline__ void lds_wait_ge(volatile LAS unsigned* word, unsigned want) {
#pragma unroll 1
  for (unsigned spins = 0; spins < (SPIN_CAP << 4); ++spins) { if (*word >= want) break; __builtin_amdgcn_s_sleep(LSLEEP); }
  asm volatile("" ::: "memory");
  SB();
}
typedef short v8s __attribute__((ext_vector_type(8)));
typedef __bf16 v8bf __attribute__((ext_vector_type(8)));
__device__ __forceinline__ v4f mfma(v4u a, v4u b, v4f c) {
  return __builtin_amdgcn_mfma_f32_16x16x32_bf16(__builtin_bit_cast(v8bf, a), __builtin_bit_cast(v8bf, b), c, 0, 0, 0);
}
__device__ __forceinline__ v4u sel4(bool on, v4u v) { return v4u{on ? v.x : 0u, on ? v.y : 0u, on ? v.z : 0u, on ? v.w : 0u}; }
__device__ __forceinline__ void tile_cols(const v4u* src, v4u (&wc)[8]) {
#pragma unroll
  for (int k = 0; k < 4; ++k) {
    wc[2 * k] = v4u{plo(src[0][k], src[1][k]), plo(src[2][k], src[3][k]), plo(src[4][k], src[5][k]), plo(src[6][k], src[7][k])};
    wc[2 * k + 1] = v4u{phi(src[0][k], src[1][k]), phi(src[2][k], src[3][k]), phi(src[4][k], src[5][k]), phi(src[6][k], src[7][k])};
    PIN(wc[2 * k]); PIN(wc[2 * k + 1]);
  }
}
__device__ __forceinline__ v4f tile_gemv(const v4u (&wc)[8], v4u hb, int lane, v4f acc) {
  const int cs = (lane >> 1) & 7;
#pragma unroll
  for (int c = 0; c < 8; ++c) acc = mfma(wc[c], sel4(cs == c, hb), acc);
  return acc;
}

struct Params {
  const u16 *x, *K, *V, *wq, *wo, *wg, *wu, *wd, *c, *wmod;
  u16* out;
  unsigned char* ws;
};

#define STAMP(i) do { } while (0)
#define DIAG_INIT() do { } while (0)
#define DIAG_FLUSH() do { } while (0)

constexpr int issued_before(int n) {
  int a = 32 + NVL + 1 + (n < 32 ? n : 31) / QDIV, b = n + WIN;
  int m = a > b ? a : b;
  return m > 64 ? 64 : m;
}
__device__ __forceinline__ void lds_inc(LAS unsigned char* L, int word, int lane) {
  ldswait();
  if (lane == 0) __hip_atomic_fetch_add((LAS unsigned*)(L + L_FLAG + word * 4), 1u, __ATOMIC_RELAXED, __HIP_MEMORY_SCOPE_WORKGROUP);
  SB();
}
constexpr v4f Z4 = {0.f, 0.f, 0.f, 0.f};

extern "C" __global__ void __launch_bounds__(NT, 3) mk_kernel(Params p) {
  extern __shared__ __attribute__((aligned(16))) unsigned char smem[];
  const u64 tstart = __builtin_amdgcn_s_memrealtime();
  asm volatile("" :: "s"(tstart));
  SB();
  LAS unsigned char* L = (LAS unsigned char*)smem;
  const int bid = blockIdx.x, t = threadIdx.x, lane = t & 63, w = __builtin_amdgcn_readfirstlane(t >> 6);
  const int x = bid & 7, j = bid >> 3;
  const int hkv = x, b = j >> 4, s = j & 15;
  const int lo16 = lane * 16, g4 = lane >> 4, n16 = lane & 15, hi3 = lane >> 3, lo3 = lane & 7;
  volatile LAS unsigned* flag = (volatile LAS unsigned*)(L + L_FLAG);
  const bool roleF = (j >= 13 && j < 16) || (j == 12 && x >= 4);
  const int f = j >= 13 ? (j - 13) * 8 + x : 20 + x;
  DIAG_INIT();
  STAMP(0);

  if (w < 4) {
    __builtin_amdgcn_s_setprio(2);
    const rsrc_t ws = mkrsrc(p.ws, WS_BYTES);
    unsigned tagv;
#define LOAD_EPOCH() __hip_atomic_load((GAS unsigned*)(p.ws + OFF_EPOCH), __ATOMIC_RELAXED, __HIP_MEMORY_SCOPE_AGENT)
    const bool roleB = j < 8;
    const bool roleA = (j >= 8 && j < 12) || (j == 12 && x < 4) || (j == 16 && (x < 2 || x >= 4));
    const bool roleS = j >= 16;
    const int e = x * 16 + s;
    const bool on = t < D / 2;
    LAS unsigned* H = (LAS unsigned*)(L + L_H + t * 4);
    if (roleA | roleB) {
      const int pitch = roleB ? HQ * DH * 2 : 6 * D * 2;
      const int unit = roleB ? x * 8 + j : j <= 12 ? x + 8 * (j - 8) : x < 2 ? 40 + x : 32 + x;
      const rsrc_t rw = mkrsrc(roleB ? p.wq : p.wmod, D * pitch);
      const int vo = (128 * w + 8 * hi3) * pitch + unit * 128 + lo3 * 16, vo2 = w < 3 ? vo + 64 * pitch : OOB;
      const rsrc_t rx = mkrsrc(roleB ? p.x : p.c, 2 * D * 2);
      const unsigned xo0 = __builtin_amdgcn_raw_buffer_load_b32(rx, on ? t * 4 : OOB, 0, 0), xo1 = __builtin_amdgcn_raw_buffer_load_b32(rx, on ? t * 4 : OOB, D * 2, 0);
      const int xv = (roleB && lane < 56) ? lo16 : OOB;
      const v4u xr0 = __builtin_amdgcn_raw_buffer_load_b128(rx, xv, 0, 0), xr1 = __builtin_amdgcn_raw_buffer_load_b128(rx, xv, D * 2, 0);
      const unsigned ep = LOAD_EPOCH();
      if (t < 8) flag[t] = 0u;
      v4u wt[16];
#pragma unroll
      for (int r = 0; r < 8; ++r) wt[r] = ldnt(rw, vo, r * pitch);
      wgbar();
#pragma unroll
      for (int r = 8; r < 16; ++r) wt[r] = ldnt(rw, vo2, (r & 7) * pitch);
      SB();
      tagv = ep + 1u;
      v4u wc0[8], wc1[8];
      if (roleA) {
        H[0] = pk2(silu(bflo(xo0)), silu(bfhi(xo0))); H[256] = pk2(silu(bflo(xo1)), silu(bfhi(xo1)));
        tile_cols(wt, wc0); tile_cols(wt + 8, wc1);
        STAMP(19);
      } else {
        float s0 = 0.f, s1 = 0.f;
#pragma unroll
        for (int k = 0; k < 4; ++k) { s0 += bflo(xr0[k]) + bfhi(xr0[k]); s1 += bflo(xr1[k]) + bfhi(xr1[k]); }
        const float m0 = wave_sum(s0) * (1.f / D), m1 = wave_sum(s1) * (1.f / D);
        float v0 = 0.f, v1 = 0.f;
#pragma unroll
        for (int k = 0; k < 4; ++k) {
          const float a = bflo(xr0[k]) - m0, c = bfhi(xr0[k]) - m0, d = bflo(xr1[k]) - m1, q = bfhi(xr1[k]) - m1;
          v0 += a * a + c * c; v1 += d * d + q * q;
        }
        if (lane >= 56) { v0 = 0.f; v1 = 0.f; }
        v0 = wave_sum(v0) * (1.f / D); v1 = wave_sum(v1) * (1.f / D);
        const float r0 = __builtin_amdgcn_rcpf(rbf(__builtin_amdgcn_sqrtf(rbf(rbf(v0) + 1e-5f)))), r1 = __builtin_amdgcn_rcpf(rbf(__builtin_amdgcn_sqrtf(rbf(rbf(v1) + 1e-5f))));
        const float mb0 = rbf(m0), mb1 = rbf(m1);
        float n00 = rbf(rbf(bflo(xo0) - mb0) * r0), n01 = rbf(rbf(bfhi(xo0) - mb0) * r0), n10 = rbf(rbf(bflo(xo1) - mb1) * r1), n11 = rbf(rbf(bfhi(xo1) - mb1) * r1);
        PIN(n00); PIN(n01); PIN(n10); PIN(n11);
        tile_cols(wt, wc0); tile_cols(wt + 8, wc1);
        STAMP(24);
        lds_wait_ge(flag + 7, 1u);
        STAMP(26);
        int off[2]; v4u g[2];
        off[0] = on ? OFF_MOD + t * 16 : OOB; off[1] = on ? OFF_MOD + D * 8 + t * 16 : OOB;
        gather<2, PSLEEP>(ws, off, g, tagv);
        STAMP(2);
        const unsigned c0 = pk2(1.f + bflo(g[1].x), 1.f + bflo(g[1].z)), c1 = pk2(1.f + bfhi(g[1].x), 1.f + bfhi(g[1].z));
        const unsigned u0 = pk2(n00 * bflo(c0), n01 * bfhi(c0)), u1 = pk2(n10 * bflo(c1), n11 * bfhi(c1));
        H[0] = on ? pk2(bflo(u0) + bflo(g[0].x), bfhi(u0) + bflo(g[0].z)) : 0u; H[256] = on ? pk2(bflo(u1) + bfhi(g[0].x), bfhi(u1) + bfhi(g[0].z)) : 0u;
      }
      ldswait();
      const LAS v4u* HA = (const LAS v4u*)(L + L_H + (lane & 1) * 1024 + (128 * w + 16 * g4 + (lane & 4) * 2) * 2);
      const v4u ha0 = HA[0], ha1 = HA[8];
      v4f y[8];
#pragma unroll
      for (int c = 0; c < 8; ++c) y[c] = mfma(ha0, wc0[c], Z4);
#pragma unroll
      for (int c = 0; c < 8; ++c) y[c] = mfma(ha1, wc1[c], y[c]);
      if (lane < 8 || (lane >= 24 && lane < 32)) {
        LAS v2f* G = (LAS v2f*)(L + L_GU + w * 1024 + (lane & 8) * 64 + lo3 * 64);
#pragma unroll
        for (int c = 0; c < 8; ++c) G[c] = v2f{y[c].x, y[c].y};
      }
      lds_inc(L, 2, lane);
      if (w == 0) {
        lds_wait_ge(flag + 2, 4u);
        const LAS v4f* G = (const LAS v4f*)(L + L_GU + (lane >> 5) * 2048 + (lane & 31) * 16);
        const v4f a = (G[0] + G[32]) + (G[64] + G[96]);
        const float e0 = roleA ? tr32(a.x, a.z) : tr32(a.x, a.y), e1 = roleA ? tr32(a.y, a.w) : tr32(a.z, a.w);
        const int so = roleA ? OFF_MOD + (64 * unit + 2 * (lane & 31) + (lane >> 5)) * 8 : OFF_Q + ((lane >> 5) * 2048 + unit * 32 + (lane & 31)) * 8;
        stsc2(ws, so, v2u{pk2(e0, e1), tagv});
        SB();
        STAMP(1);
      }
    } else {
      const unsigned ep = LOAD_EPOCH();
      if (t < 8) flag[t] = 0u;
      wgbar();
      tagv = ep + 1u;
    }
    v4u ec0[8], ec1[8];
    const int vd = (w * 16 + n16 < NCG) ? g4 * 8 * D * 2 + w * 256 + n16 * 16 : OOB;
#define LOAD_LAST() { \
      const rsrc_t rw = mkrsrc(roleS ? p.wo + (size_t)e * 32 * D : p.wd + (size_t)f * 64 * D, roleS ? 32 * D * 2 : 64 * D * 2); \
      v4u wt[16]; \
      _Pragma("unroll") for (int k = 0; k < 8; ++k) wt[k] = ldnt(rw, vd, k * D * 2); \
      _Pragma("unroll") for (int k = 8; k < 16; ++k) wt[k] = ldnt(rw, roleS ? OOB : vd, (24 + k) * D * 2); \
      SB(); \
      tile_cols(wt, ec0); tile_cols(wt + 8, ec1); }
    if (roleS || (DEARLY && roleF)) LOAD_LAST()
    const unsigned tag = __builtin_amdgcn_readfirstlane(tagv);
    if (w == 1) {
      u64 pc; unsigned back, l1, l2, l3;
      asm volatile("s_getpc_b64 %0\n.Lmk_anchor:\n\ts_mov_b32 %1, .Lmk_anchor-mk_kernel\n\ts_mov_b32 %2, .Lmk_end1-mk_kernel\n\ts_mov_b32 %3, .Lmk_end2-mk_kernel\n\ts_mov_b32 %4, .Lmk_end3-mk_kernel"
                   : "=s"(pc), "=s"(back), "=s"(l1), "=s"(l2), "=s"(l3));
      const unsigned len = max(l1, max(l2, l3));
      const unsigned co = min((unsigned)(j * 8 + lo3) * 128u, (len - 4u) & ~3u);
      const unsigned cpf = *(const GAS unsigned*)(pc - back + co);
      asm volatile("" :: "v"(cpf));
    }
    if (w == 3) {
      lds_wait_ge(flag + 1, 1u);
      STAMP(25);
      {
        int off[2]; v4u g[2];
        off[0] = OFF_Q + (b * 2048 + hkv * 256) * 8 + lo16; off[1] = off[0] + 1024;
        gather<2, PSLEEP>(ws, off, g, tag);
        *(LAS v2u*)(L + L_Q + lane * 8) = v2u{g[0].x, g[0].z}; *(LAS v2u*)(L + L_Q + 512 + lane * 8) = v2u{g[1].x, g[1].z};
        lds_inc(L, 0, lane);
      }
      STAMP(4);
      lds_wait_ge(flag + 3, 8u);
      const int h = g4 & 1;
      const v2f ml = *(const LAS v2f*)(L + L_ML + (h * 8 + lo3) * 8);
      float M = ml.x;
      M = fmaxf(M, dpp<XOR1>(M)); M = fmaxf(M, dpp<XOR2>(M)); M = fmaxf(M, dpp<XOR7>(M));
      float Ls = __builtin_amdgcn_exp2f(ml.x - M) * ml.y;
      Ls += dpp<XOR1>(Ls); Ls += dpp<XOR2>(Ls); Ls += dpp<XOR7>(Ls);
      const int mo = OFF_ML + (b * 8 + hkv) * 2048 + h * 16;
      stsc(ws, (lane == 0 || lane == 16) ? mo + s * 128 : OOB, v4u{__float_as_uint(M), tag, __float_as_uint(Ls), tag});
      waitvm<0>();
      lds_wait_ge(flag + 6, 1u);
      int off[1]; v4u g[1];
      off[0] = mo + n16 * 128;
      gather<1, PSLEEP>(ws, off, g, tag);
      float G = __uint_as_float(g[0].x);
      G = fmaxf(G, dpp<XOR1>(G)); G = fmaxf(G, dpp<XOR2>(G)); G = fmaxf(G, dpp<ROR4>(G)); G = fmaxf(G, dpp<ROR8>(G));
      float T = __builtin_amdgcn_exp2f(__uint_as_float(g[0].x) - G) * __uint_as_float(g[0].z);
      T += dpp<XOR1>(T); T += dpp<XOR2>(T); T += dpp<ROR4>(T); T += dpp<ROR8>(T);
      if (lane < 32 && !(lane & 8)) *(LAS float*)(L + L_FW + (h * 8 + lo3) * 4) = __builtin_amdgcn_exp2f(ml.x - G) / T;
      lds_inc(L, 5, lane);
      STAMP(3);
    }
    lds_wait_ge(flag + 5, 1u);
    {
      const int h = w >> 1, dp = t & 127;
      v4f f0 = *(const LAS v4f*)(L + L_FW + h * 32), f1 = *(const LAS v4f*)(L + L_FW + h * 32 + 16);
      int po = OFF_PO + ((((2 * hkv + h) * 8 + (dp >> 4)) * 2 + b) * 16 + s) * 256 + (dp & 15) * 16;
      PIN(f0); PIN(f1); PIN(po);
      wgbar();
      const LAS v2f* O = (const LAS v2f*)(L + L_O + h * 1024 + dp * 8);
      v2f v[8];
#pragma unroll
      for (int k = 0; k < 8; ++k) v[k] = O[k * 256];
      SB();
      const v2f o = ((v[0] * f0.x + v[1] * f0.y) + (v[2] * f0.z + v[3] * f0.w)) + ((v[4] * f1.x + v[5] * f1.y) + (v[6] * f1.z + v[7] * f1.w));
      stsc(ws, po, v4u{__float_as_uint(o.x), tag, __float_as_uint(o.y), tag});
      SB();
    }
    STAMP(9);
    const int cg0 = 16 * w + 4 * g4;

    if (roleS) {
      int off[2]; v4u g[2];
      off[0] = OFF_PO + e * 8192 + (lane & 2) * 2048 + (lane >> 2) * 256 + w * 64 + (lane & 1) * 32; off[1] = off[0] + 16;
      int so = cg0 < NCG ? OFF_Y1 + (cg0 * N1 + e) * 128 + n16 * 8 : OOB;
      PIN(so);
      gather<2>(ws, off, g, tag);
      STAMP(10);
      float v = tr16(tr32(__uint_as_float(g[0].x), __uint_as_float(g[1].x)), tr32(__uint_as_float(g[0].z), __uint_as_float(g[1].z)));
      v += dpp<ROR8>(v); v += dpp<ROR4>(v);
      if (n16 < 4) *(LAS u16*)(L + L_S1 + (lane & 2) * 32 + (w * 8 + (lane & 1) * 4 + g4) * 2) = (u16)pk2(v, v);
      wgbar();
      const v4u ob = *(const LAS v4u*)(L + L_S1 + (lane & 1) * 64 + g4 * 16);
      const v4f y = tile_gemv(ec0, ob, lane, Z4);
#pragma unroll
      for (int k = 0; k < 4; ++k) __builtin_amdgcn_raw_buffer_store_b64(v2u{__float_as_uint(y[k]), tag}, ws, so, k * N1 * 128, AUX_SC1);
      STAMP(11);
      asm volatile(".Lmk_end1:");
    } else if (roleF) {
      int om[2]; v4u gm[2];
      om[0] = on ? OFF_MOD + 3 * D * 8 + t * 16 : OOB; om[1] = on ? OFF_MOD + 4 * D * 8 + t * 16 : OOB;
      gm[0] = ldsc(ws, om[0]); gm[1] = ldsc(ws, om[1]);
      wgbar();
      if (!DEARLY) LOAD_LAST()
      if (!__all(((gm[0].y == tag) & (gm[0].w == tag) & (gm[1].y == tag) & (gm[1].w == tag)) | !on)) gather<2>(ws, om, gm, tag);
      float sb[4] = {bflo(gm[0].x), bfhi(gm[0].x), bflo(gm[0].z), bfhi(gm[0].z)};
      float cb[4] = {rbf(1.f + bflo(gm[1].x)), rbf(1.f + bfhi(gm[1].x)), rbf(1.f + bflo(gm[1].z)), rbf(1.f + bfhi(gm[1].z))};
#pragma unroll
      for (int k = 0; k < 4; ++k) { PIN(sb[k]); PIN(cb[k]); }
      int so = cg0 < NCG ? OFF_Y2 + (cg0 * N2 + f) * 128 + n16 * 8 : OOB;
      PIN(so);
      int ox[2]; v4u gx[2];
      ox[0] = on ? OFF_X1 + (t >> 2) * 128 + (t & 3) * 16 : OFF_X1; ox[1] = OFF_X1;
      gather<2>(ws, ox, gx, tag);
      STAMP(15);
      if (!on) gx[0].z = gx[0].x;
      const float xa0 = bflo(gx[0].x), xb0 = bfhi(gx[0].x), xa1 = bflo(gx[0].z), xb1 = bfhi(gx[0].z), pa = bflo(gx[1].x), pb = bfhi(gx[1].x);
      const float da0 = xa0 - pa, db0 = xb0 - pb, da1 = xa1 - pa, db1 = xb1 - pb;
      float u = tr16(tr32(da0 + da1, da0 * da0 + da1 * da1), tr32(db0 + db1, db0 * db0 + db1 * db1));
      u += dpp<XOR1>(u); u += dpp<XOR2>(u); u += dpp<ROR4>(u); u += dpp<ROR8>(u);
      if (n16 == 0) *(LAS float*)(L + L_ST + w * 16 + g4 * 4) = u;
      wgbar();
      const LAS v4f* ST = (const LAS v4f*)(L + L_ST);
      const v4f ss = ((ST[0] + ST[1]) + (ST[2] + ST[3])) * (1.f / D);
      const unsigned vr = pk2(fmaxf(ss.z - ss.x * ss.x, 0.f), fmaxf(ss.w - ss.y * ss.y, 0.f)), mn = pk2(pa + ss.x, pb + ss.y);
      const unsigned ve = pk2(bflo(vr) + 1e-5f, bfhi(vr) + 1e-5f);
      const unsigned sd = pk2(__builtin_amdgcn_sqrtf(bflo(ve)), __builtin_amdgcn_sqrtf(bfhi(ve)));
      const float ra = __builtin_amdgcn_rcpf(bflo(sd)), rb = __builtin_amdgcn_rcpf(bfhi(sd));
      const unsigned ca = pk2(xa0 - bflo(mn), xa1 - bflo(mn)), cc = pk2(xb0 - bfhi(mn), xb1 - bfhi(mn));
      const unsigned na = pk2(bflo(ca) * ra, bfhi(ca) * ra), nb = pk2(bflo(cc) * rb, bfhi(cc) * rb);
      const unsigned ma = pk2(bflo(na) * cb[0], bfhi(na) * cb[2]), mb = pk2(bflo(nb) * cb[1], bfhi(nb) * cb[3]);
      H[0] = pk2(bflo(ma) + sb[0], bfhi(ma) + sb[2]); H[256] = pk2(bflo(mb) + sb[1], bfhi(mb) + sb[3]);
      wgbar();
      wgbar();
      v4f gw[7];
#pragma unroll
      for (int k = 0; k < 7; ++k) gw[k] = *(const LAS v4f*)(L + L_GU + k * 1024 + lo16);
      SB();
      const v4f a = ((gw[0] + gw[1]) + (gw[2] + gw[3])) + ((gw[4] + gw[5]) + gw[6]);
      const unsigned gb = pk2(a.x, a.y), ub = pk2(a.z, a.w), sg = pk2(silu(bflo(gb)), silu(bfhi(gb)));
      const unsigned ac = pk2(bflo(sg) * bflo(ub), bfhi(sg) * bfhi(ub));
      *(LAS u16*)(L + L_ACT + w * 256 + lane * 2) = (u16)ac; *(LAS u16*)(L + L_ACT + w * 256 + 128 + lane * 2) = (u16)(ac >> 16);
      ldswait();
      const LAS v4u* E = (const LAS v4u*)(L + L_ACT + w * 256 + (lane & 1) * 128 + g4 * 16);
      const v4u e0 = E[0], e1 = E[4];
      const v4f y = tile_gemv(ec0, e0, lane, Z4) + tile_gemv(ec1, e1, lane, Z4);
#pragma unroll
      for (int k = 0; k < 4; ++k) __builtin_amdgcn_raw_buffer_store_b64(v2u{__float_as_uint(y[k]), tag}, ws, so, k * N2 * 128, AUX_SC1);
      STAMP(16);
      asm volatile(".Lmk_end2:");
    } else if (j < 7) {
      const int cg = j * 8 + x;
      v2u gm[2] = {{0, 0}, {0, 0}}; unsigned xin0 = 0, xin1 = 0;
      if (w == 0) {
        xin0 = p.x[8 * cg + lo3]; xin1 = p.x[D + 8 * cg + lo3];
        int om[2]; om[0] = OFF_MOD + (2 * D + 8 * cg + lo3) * 8; om[1] = OFF_MOD + (5 * D + 8 * cg + lo3) * 8;
        gather2<2>(ws, om, gm, tag);
        PIN(xin0); PIN(xin1);
      }
      int of[4]; v4u gg[4];
#pragma unroll
      for (int k = 0; k < 4; ++k) of[k] = OFF_Y1 + cg * N1 * 128 + k * 4096 + t * 16;
      gather<4>(ws, of, gg, tag);
      STAMP(12);
      const float a0 = (__uint_as_float(gg[0].x) + __uint_as_float(gg[1].x)) + (__uint_as_float(gg[2].x) + __uint_as_float(gg[3].x));
      const float a1 = (__uint_as_float(gg[0].z) + __uint_as_float(gg[1].z)) + (__uint_as_float(gg[2].z) + __uint_as_float(gg[3].z));
      float v2 = tr32(a0, a1); v2 = tr16(v2, v2); v2 += dpp<ROR8>(v2);
      if ((lane & 24) == 0) *(LAS float*)(L + L_RED + (w * 16 + lo3 * 2 + (lane >> 5)) * 4) = v2;
      wgbar();
      if (w == 0) {
        const LAS v2f* R = (const LAS v2f*)(L + L_RED + lo3 * 8);
        const v2f y1 = (R[0] + R[8]) + (R[16] + R[24]);
        const unsigned yb = pk2(y1.x, y1.y), gy = pk2(bflo(gm[0].x) * bflo(yb), bfhi(gm[0].x) * bfhi(yb));
        const unsigned x1v = pk2(bflo(xin0) + bflo(gy), bflo(xin1) + bfhi(gy));
        stsc2(ws, lane < 8 ? OFF_X1 + cg * 128 + lane * 8 : OOB, v2u{x1v, tag});
        SB();
        STAMP(13);
        int o2[4]; v4u g2[4];
#pragma unroll
        for (int k = 0; k < 4; ++k) o2[k] = (k * 8 + hi3 < N2) ? OFF_Y2 + cg * N2 * 128 + k * 1024 + lo16 : OOB;
        gather<4>(ws, o2, g2, tag);
        STAMP(17);
        const float c0 = (__uint_as_float(g2[0].x) + __uint_as_float(g2[1].x)) + (__uint_as_float(g2[2].x) + __uint_as_float(g2[3].x));
        const float c1 = (__uint_as_float(g2[0].z) + __uint_as_float(g2[1].z)) + (__uint_as_float(g2[2].z) + __uint_as_float(g2[3].z));
        float y2 = tr32(c0, c1); y2 = tr16(y2, y2); y2 += dpp<ROR8>(y2);
        const bool hb = lane >= 32;
        const float res = (hb ? bfhi(x1v) : bflo(x1v)) + rbf((hb ? bfhi(gm[1].x) : bflo(gm[1].x)) * rbf(y2));
        while (__builtin_amdgcn_s_memrealtime() - tstart < 5000) { }
        if ((lane & 24) == 0) p.out[(lane >> 5) * D + 8 * cg + lo3] = (u16)pk2(res, res);
        if (cg == 0 && lane == 0) __hip_atomic_store((GAS unsigned*)(p.ws + OFF_EPOCH), tag, __ATOMIC_RELAXED, __HIP_MEMORY_SCOPE_AGENT);
        STAMP(18);
        asm volatile(".Lmk_end3:");
      }
    }
  } else {
    const int sw = w - 4;
    const size_t kvoff = ((size_t)(b * HKV + hkv) * SK + s * 512 + sw * 64) * DH;
    const rsrc_t rK = mkrsrc(p.K + kvoff, 64 * DH * 2), rV = mkrsrc(p.V + kvoff, 64 * DH * 2);
    const int vk = lo3 * 512 + (lane & 8) * 8 + g4 * 16;
    const int vv = g4 * 512 + n16 * 16;
    v4u kv[64];
#define LOADKV(i) kv[i] = (i) < 32 ? ldnt(rK, vk, ((i) >> 2) * 4096 + ((i) & 3) * 128) : ldnt(rV, vv, (((i) - 32) >> 1) * 2048 + ((i) & 1) * 256)
#define ISSUE_TO(FROM, TO) _Pragma("unroll") for (int i_ = (FROM); i_ < (TO); ++i_) { LOADKV(i_); SB(); }
    ISSUE_TO(0, TD)
#pragma unroll
    for (int i = TD; i < 32; ++i) {
      waitvm<TD - 1>(); LOADKV(i); SB();
      if (i == BAR_AT) wgbar();
      if (i == MPOLL_AT) lds_inc(L, 7, lane);
      if (i == QPOLL_AT) lds_inc(L, 1, lane);
    }
    {
      const unsigned lb = (unsigned)(size_t)(L + L_V) + sw * NVL * 1024;
#pragma unroll
      for (int i = 0; i < NVL; ++i) {
        waitvm<TD - 1>();
        unsigned keep;
        asm volatile("s_mov_b32 %0, m0\n\ts_mov_b32 m0, %1\n\ts_nop 0\n\tbuffer_load_dwordx4 %2, %3, %4 offen nt lds\n\ts_mov_b32 m0, %0"
                     : "=&s"(keep) : "s"(lb + i * 1024), "v"(vv), "s"(rV), "s"((i >> 1) * 2048 + (i & 1) * 256) : "memory");
        SB();
        if (32 + i == QPOLL_AT) lds_inc(L, 1, lane);
      }
    }
    STAMP(5);
    lds_wait_ge(flag, 1u);
    v4u qa[4];
#pragma unroll
    for (int st = 0; st < 4; ++st) qa[st] = *(const LAS v4u*)(L + L_Q + (lane & 1) * 512 + st * 128 + (lane & 2) * 32 + g4 * 16);
    STAMP(6);
    float ev0 = 0.f, ev1 = 0.f, ov0 = 0.f, ov1 = 0.f;
    const bool c2 = lane & 8;
#pragma unroll
    for (int tt = 0; tt < 8; ++tt) {
      v4f a = Z4;
#pragma unroll
      for (int st = 0; st < 4; ++st) {
        const int n = tt * 4 + st;
        ISSUE_TO(n == 0 ? 32 + NVL : issued_before(n - 1), issued_before(n))
        a = mfma(qa[st], kv[n], a);
        SB();
      }
      const float v0 = c2 ? a.z : a.x, v1 = c2 ? a.w : a.y;
      const bool mine = g4 == (tt >> 1);
      if (tt & 1) { ov0 = mine ? v0 : ov0; ov1 = mine ? v1 : ov1; } else { ev0 = mine ? v0 : ev0; ev1 = mine ? v1 : ev1; }
    }
    const float sc0 = rbf(trd<ROR8>(ev0, ov0, c2)) * (0.0625f * 1.4426950408889634f);
    const float sc1 = rbf(trd<ROR8>(ev1, ov1, c2)) * (0.0625f * 1.4426950408889634f);
    {
      auto sx = __builtin_amdgcn_permlane32_swap(__float_as_uint(sc0), __float_as_uint(sc1), false, false);
      float m = fmaxf(__uint_as_float(sx[0]), __uint_as_float(sx[1]));
      m = max16(m); m = fmaxf(m, dpp<ROR8>(m)); m = fmaxf(m, dpp<ROR4>(m)); m = fmaxf(m, dpp<XOR2>(m)); m = fmaxf(m, dpp<XOR1>(m));
      const float m0 = __builtin_bit_cast(float, __builtin_amdgcn_readlane(__builtin_bit_cast(int, m), 0));
      const float m1 = __builtin_bit_cast(float, __builtin_amdgcn_readlane(__builtin_bit_cast(int, m), 32));
      const float p0 = __builtin_amdgcn_exp2f(sc0 - m0), p1 = __builtin_amdgcn_exp2f(sc1 - m1);
      float l = tr32(p0, p1);
      l = tr16(l, l); l += dpp<ROR8>(l); l += dpp<ROR4>(l); l += dpp<XOR2>(l); l += dpp<XOR1>(l);
      const unsigned pp = pk2(p0, p1);
      LAS unsigned* P = (LAS unsigned*)(L + L_P + sw * 1024 + (lane & 3) * 64 + (lane >> 2) * 4);
      P[0] = pp & 0xffffu; P[64] = pp << 16; P[128] = pp >> 16; P[192] = pp & 0xffff0000u;
      if ((lane & 31) == 0) *(LAS v2f*)(L + L_ML + ((lane >> 5) * 8 + sw) * 8) = v2f{m, l};
    }
    lds_inc(L, 3, lane);
    waitvm<issued_before(31) - 32 - NVL>();
#pragma unroll
    for (int i = 0; i < NVL; ++i) kv[32 + i] = *(const LAS v4u*)(L + L_V + sw * NVL * 1024 + i * 1024 + lo16);
    v4u pr[4];
#pragma unroll
    for (int k = 0; k < 4; ++k) pr[k] = *(const LAS v4u*)(L + L_P + sw * 1024 + (lane & 8) * 64 + (lane & 1) * 256 + g4 * 64 + k * 16);
    STAMP(7);
    v4f acc0 = Z4, acc1 = Z4;
    const int jq = (lane >> 1) & 3;
    unsigned fpf = 0;
#pragma unroll
    for (int ip = 0; ip < 16; ++ip) {
      ISSUE_TO(issued_before(32 + 2 * ip - 1), issued_before(32 + 2 * ip + 1))
      if (ip == ML_AT) lds_inc(L, 6, lane);
      if (FPF && ip == FPF) {
        const int idx = (j * 8 + sw) * 14 + min(lane, 13), k = idx / 896, r = idx - k * 896, fk = k < 3 ? k * 8 + x : x >= 4 ? 20 + x : x;
        fpf = __builtin_nontemporal_load((const GAS unsigned*)((r < D ? p.wg + (size_t)r * FF : p.wu + (size_t)(r - D) * FF) + fk * 64));
        SB();
      }
      const unsigned pu = pr[ip >> 2][ip & 3];
      const v4u pa = {jq == 0 ? pu : 0u, jq == 1 ? pu : 0u, jq == 2 ? pu : 0u, jq == 3 ? pu : 0u};
      acc0 = mfma(pa, kv[32 + 2 * ip], acc0); acc1 = mfma(pa, kv[33 + 2 * ip], acc1);
      SB();
    }
    LAS v4f* O = (LAS v4f*)(L + L_O + sw * 2048 + (g4 >> 1) * 1024 + n16 * 32 + (g4 & 1) * 16);
    O[0] = acc0; O[32] = acc1;
    STAMP(8);
    wgbar();
    if (FPF) asm volatile("" :: "v"(fpf));
    if (roleF && sw < 7) {
      const GAS v4u* gu = (const GAS v4u*)(((lane & 8) ? p.wu : p.wg) + (size_t)(64 * sw + 8 * g4) * FF + f * 64 + lo3 * 8);
      wgbar();
      v4u wt[16];
#pragma unroll
      for (int k = 0; k < 16; ++k) wt[k] = __builtin_nontemporal_load(gu + ((k >> 3) * 32 + (k & 7)) * (FF / 8));
      SB();
      v4u gc0[8], gc1[8];
      tile_cols(wt, gc0); tile_cols(wt + 8, gc1);
      STAMP(14);
      wgbar();
      wgbar();
      const LAS v4u* HA = (const LAS v4u*)(L + L_H + (lane & 1) * 1024 + (64 * sw + 8 * g4) * 2);
      const v4u h0 = HA[0], h1 = HA[4];
      v4f y[8];
#pragma unroll
      for (int c = 0; c < 8; ++c) y[c] = mfma(h0, gc0[c], Z4);
#pragma unroll
      for (int c = 0; c < 8; ++c) y[c] = mfma(h1, gc1[c], y[c]);
      if (lane < 16) {
        LAS v2f* G = (LAS v2f*)(L + L_GU + sw * 1024 + lo3 * 128 + (lane & 8));
#pragma unroll
        for (int c = 0; c < 8; ++c) G[2 * c] = v2f{y[c].x, y[c].y};
      }
      wgbar();
    }
  }
  DIAG_FLUSH();
}

extern "C" void kernel_launch(void* const* d_in, const int* in_sizes, int n_in, void* d_out, int out_size, void* d_ws, size_t ws_size, hipStream_t stream) {
  Params p;
  p.x = (const u16*)d_in[0]; p.K = (const u16*)d_in[1]; p.V = (const u16*)d_in[2]; p.wq = (const u16*)d_in[3]; p.wo = (const u16*)d_in[4];
  p.wg = (const u16*)d_in[6]; p.wu = (const u16*)d_in[7]; p.wd = (const u16*)d_in[8]; p.c = (const u16*)d_in[9]; p.wmod = (const u16*)d_in[10];
  p.out = (u16*)d_out; p.ws = (unsigned char*)d_ws;
  (void)hipFuncSetAttribute((const void*)mk_kernel, hipFuncAttributeMaxDynamicSharedMemorySize, LDS_BYTES);
  hipLaunchKernelGGL(mk_kernel, dim3(NWG), dim3(NT), LDS_BYTES, stream, p);
}
```

```cpp
#include <hip/hip_runtime.h>
#include <hip/hip_bf16.h>
#include <cstdint>

#define DIAG 0

typedef unsigned short u16;
typedef unsigned long long u64;
typedef unsigned v4u __attribute__((ext_vector_type(4)));
typedef unsigned v2u __attribute__((ext_vector_type(2)));
typedef float v4f __attribute__((ext_vector_type(4)));
typedef float v2f __attribute__((ext_vector_type(2)));
typedef __bf16 bf16x2 __attribute__((ext_vector_type(2)));
#define LAS __attribute__((address_space(3)))
#define GAS __attribute__((address_space(1)))
typedef __amdgpu_buffer_rsrc_t rsrc_t;

constexpr int D = 448, HQ = 16, HKV = 8, SK = 8192, DH = 256, FF = 1792;
constexpr int NWG = 256, NT = 768;
constexpr int N1 = 128;
constexpr int N2 = 28;
constexpr int NCG = 56;
constexpr int OFF_EPOCH = 0;
constexpr int OFF_PREF = 512;
constexpr int OFF_MOD = 1024;
constexpr int OFF_Q = 24576;
constexpr int OFF_ML = 65536;
constexpr int OFF_PRB = 98304;
constexpr int OFF_X1 = 114688;
constexpr int OFF_Y1 = 131072;
constexpr int OFF_Y2 = OFF_Y1 + 56 * 16384;
constexpr int OFF_PO = 2 << 20;
constexpr int OFF_PRB1 = OFF_PO + 64 * 16384;
constexpr int WS_BYTES = OFF_PRB1 + 16384;
static_assert(OFF_Y2 + 16 * 16384 <= OFF_PO, "workspace map");
__device__ __forceinline__ int po_base(int e, unsigned near) { return OFF_PO + (e & 63) * 16384 + ((near >> (8 + (e >> 4))) & 1) * 8192; }
__device__ __forceinline__ int y1_line(int cg, int e, unsigned near) { return OFF_Y1 + (((cg >> 3) * 4 + (cg & 3)) * 2 + (e >> 6)) * 16384 + ((near >> (cg & 7)) & 1) * 8192 + (e & 63) * 128; }
__device__ __forceinline__ int y2_line(int cg, int f, unsigned near) { return OFF_Y2 + ((cg >> 4) * 4 + (cg & 3)) * 16384 + ((near >> (cg & 7)) & 1) * 8192 + ((cg >> 3) & 1) * 4096 + f * 128; }
constexpr int OFF_DIAG = 8 << 20;
constexpr int AUX_NT = 2, AUX_SC1 = 16;
constexpr unsigned SPIN_CAP = 1u << 20;
constexpr int OOB = 0x7ffffff0;

#define TD 4
#define WIN 4
#ifndef NVL
#define NVL 4
#endif
#define BAR_AT 0
#define MPOLL_AT 13
#define QPOLL_AT 24
#define ML_AT 9
#define QDIV 6
#define CODEPF 2
#define FPF 0
#define DEARLY 1
#define FWAIT 7u
#define TEND 18600
#define P0H 1900
#define P0L 0
#define TAILN 8u
#define FIRST 3
#define GU0 3500
#define GUTAU 500
#define STAG 1
#define NGE 14
#define NGT 7
#define TMOD 5300
#define TQ 8200
#define CATCH 20
#define BURST 2
#define ROT 1
#define ANCHOR 0
#define TLBW 0
#define HOLD1 0
#define HOLD2 0
#define HOLD3 0
#define HOLD4 0
#define PRET 0
#define PROBE 0
#define NPROBE 4
#define FLIP 0
#define TLANDH 2600
#define TLANDL 1300
#define GALL 0
#define LSLEEP 1
#define PSLEEP 8

constexpr int L_FLAG = 0;
constexpr int L_Q = 64;
constexpr int L_ML = 1280;
constexpr int L_FW = 1408;
constexpr int L_ST = 2048;
constexpr int L_S1 = 1664;
constexpr int L_RED = 1792;
constexpr int L_H = 2048;
constexpr int L_ACT = 4096;
constexpr int L_DIAG = 5120;
constexpr int L_P = 8192;
constexpr int L_GU = L_P;
constexpr int L_V = 16384;
constexpr int L_W = 49152;
constexpr int LDS_BYTES = L_W + 112 * 1024;
static_assert(NGE >= 14 && NVL <= 4 && LDS_BYTES <= 160 * 1024, "LDS map");

__device__ __forceinline__ float bflo(unsigned v) { return __uint_as_float(v << 16); }
__device__ __forceinline__ float bfhi(unsigned v) { return __uint_as_float(v & 0xffff0000u); }
__device__ __forceinline__ unsigned pk2(float lo, float hi) { bf16x2 v = {(__bf16)lo, (__bf16)hi}; return __builtin_bit_cast(unsigned, v); }
__device__ __forceinline__ float rbf(float f) { return (float)(__bf16)f; }
__device__ __forceinline__ float dot2(unsigned a, unsigned b, float acc) {
  return __builtin_amdgcn_fdot2_f32_bf16(__builtin_bit_cast(bf16x2, a), __builtin_bit_cast(bf16x2, b), acc, false);
}
__device__ __forceinline__ unsigned plo(unsigned a, unsigned b) { return __builtin_amdgcn_perm(b, a, 0x05040100u); }
__device__ __forceinline__ unsigned phi(unsigned a, unsigned b) { return __builtin_amdgcn_perm(b, a, 0x07060302u); }
__device__ __forceinline__ float silu(float x) { return x * __builtin_amdgcn_rcpf(1.f + __builtin_amdgcn_exp2f(x * -1.4426950408889634f)); }
template <int CTRL> __device__ __forceinline__ float dpp(float x) {
  return __builtin_bit_cast(float, __builtin_amdgcn_mov_dpp(__builtin_bit_cast(int, x), CTRL, 0xf, 0xf, true));
}
constexpr int XOR1 = 0xB1, XOR2 = 0x4E, XOR7 = 0x141, ROR8 = 0x128, ROR4 = 0x124;
__device__ __forceinline__ float tr32(float a, float b) {
  auto s = __builtin_amdgcn_permlane32_swap(__float_as_uint(a), __float_as_uint(b), false, false);
  return __uint_as_float(s[0]) + __uint_as_float(s[1]);
}
__device__ __forceinline__ float tr16(float a, float b) {
  auto s = __builtin_amdgcn_permlane16_swap(__float_as_uint(a), __float_as_uint(b), false, false);
  return __uint_as_float(s[0]) + __uint_as_float(s[1]);
}
template <int CTRL> __device__ __forceinline__ float trd(float a, float b, bool bit) {
  const float keep = bit ? b : a, send = bit ? a : b;
  return keep + dpp<CTRL>(send);
}
__device__ __forceinline__ float max32(float x) {
  auto s = __builtin_amdgcn_permlane32_swap(__float_as_uint(x), __float_as_uint(x), false, false);
  return fmaxf(__uint_as_float(s[0]), __uint_as_float(s[1]));
}
__device__ __forceinline__ float max16(float x) {
  auto s = __builtin_amdgcn_permlane16_swap(__float_as_uint(x), __float_as_uint(x), false, false);
  return fmaxf(__uint_as_float(s[0]), __uint_as_float(s[1]));
}
__device__ __forceinline__ float wave_sum(float x) {
  x += dpp<XOR1>(x); x += dpp<XOR2>(x); x += dpp<ROR4>(x); x += dpp<ROR8>(x); x = tr16(x, x); return tr32(x, x);
}
__device__ __forceinline__ rsrc_t mkrsrc(const void* p, int bytes) { return __builtin_amdgcn_make_buffer_rsrc((void*)p, 0, bytes, 0x00020000); }
__device__ __forceinline__ v4u ldnt(rsrc_t r, int voff, int soff) { return __builtin_amdgcn_raw_buffer_load_b128(r, voff, soff, AUX_NT); }
__device__ __forceinline__ v4u ldsc(rsrc_t r, int voff) { return __builtin_amdgcn_raw_buffer_load_b128(r, voff, 0, AUX_SC1); }
__device__ __forceinline__ v2u ldsc2(rsrc_t r, int voff) { return __builtin_amdgcn_raw_buffer_load_b64(r, voff, 0, AUX_SC1); }
__device__ __forceinline__ void stsc(rsrc_t r, int voff, v4u v) { __builtin_amdgcn_raw_buffer_store_b128(v, r, voff, 0, AUX_SC1); }
__device__ __forceinline__ void stsc2(rsrc_t r, int voff, v2u v) { __builtin_amdgcn_raw_buffer_store_b64(v, r, voff, 0, AUX_SC1); }
template <int N> __device__ __forceinline__ void waitvm() { asm volatile("s_waitcnt vmcnt(%0)" :: "i"(N) : "memory"); }
#define SB() __builtin_amdgcn_sched_barrier(0)
#define PIN(x) asm volatile("" : "+v"(x))
__device__ __forceinline__ void ldswait() { asm volatile("s_waitcnt lgkmcnt(0)" ::: "memory"); }
__device__ __forceinline__ void wgbar() { SB(); asm volatile("s_waitcnt lgkmcnt(0)" ::: "memory"); __builtin_amdgcn_s_barrier(); asm volatile("" ::: "memory"); SB(); }

template <int N, int SLP = 0> __device__ __forceinline__ void gather(rsrc_t ws, int (&off)[N], v2u (&val)[N], unsigned tag) {
#pragma unroll
  for (int k = 0; k < N; ++k) val[k] = v2u{0u, 0u};
#pragma unroll 1
  for (unsigned spins = 0; spins < SPIN_CAP; ++spins) {
    v4u g[N];
#pragma unroll
    for (int k = 0; k < N; ++k) g[k] = ldsc(ws, off[k]);
    bool open = false;
#pragma unroll
    for (int k = 0; k < N; ++k) {
      const bool m = (g[k].y == tag) & (g[k].w == tag);
      val[k].x = m ? g[k].x : val[k].x; val[k].y = m ? g[k].z : val[k].y; off[k] = m ? OOB : off[k];
      open |= off[k] != OOB;
    }
    if (!__any(open)) break;
    if (SLP) __builtin_amdgcn_s_sleep(SLP);
    asm volatile("" ::: "memory");
  }
}
template <int N> __device__ __forceinline__ void gather_all(rsrc_t ws, const int (&off)[N], v2u (&val)[N], unsigned tag, bool skip = false) {
#pragma unroll 1
  for (unsigned spins = 0; spins < SPIN_CAP; ++spins) {
    v4u g[N];
#pragma unroll
    for (int k = 0; k < N; ++k) g[k] = ldsc(ws, off[k]);
    bool ok = true;
#pragma unroll
    for (int k = 0; k < N; ++k) { ok &= ((g[k].y == tag) & (g[k].w == tag)) | (k == N - 1 && skip); val[k] = v2u{g[k].x, g[k].z}; }
    if (__all(ok)) break;
    asm volatile("" ::: "memory");
  }
}
template <int N> __device__ __forceinline__ void gather2(rsrc_t ws, const int (&off)[N], v2u (&g)[N], unsigned tag) {
#pragma unroll 1
  for (unsigned spins = 0; spins < SPIN_CAP; ++spins) {
    bool ok = true;
#pragma unroll
    for (int k = 0; k < N; ++k) g[k] = ldsc2(ws, off[k]);
#pragma unroll
    for (int k = 0; k < N; ++k) ok &= (g[k].y == tag) | (off[k] == OOB);
    if (__all(ok)) break;
    asm volatile("" ::: "memory");
  }
}
__device__ __forceinline__ void lds_wait_ge(volatile LAS unsigned* word, unsigned want) {
#pragma unroll 1
  for (unsigned spins = 0; spins < (SPIN_CAP << 4); ++spins) { if (*word >= want) break; __builtin_amdgcn_s_sleep(LSLEEP); }
  asm volatile("" ::: "memory");
  SB();
}
typedef short v8s __attribute__((ext_vector_type(8)));
typedef __bf16 v8bf __attribute__((ext_vector_type(8)));
__device__ __forceinline__ v4f mfma(v4u a, v4u b, v4f c) {
  return __builtin_amdgcn_mfma_f32_16x16x32_bf16(__builtin_bit_cast(v8bf, a), __builtin_bit_cast(v8bf, b), c, 0, 0, 0);
}
__device__ __forceinline__ v4u sel4(bool on, v4u v) { return v4u{on ? v.x : 0u, on ? v.y : 0u, on ? v.z : 0u, on ? v.w : 0u}; }
__device__ __forceinline__ void tile_cols(const v4u* src, v4u (&wc)[8]) {
#pragma unroll
  for (int k = 0; k < 4; ++k) {
    wc[2 * k] = v4u{plo(src[0][k], src[1][k]), plo(src[2][k], src[3][k]), plo(src[4][k], src[5][k]), plo(src[6][k], src[7][k])};
    wc[2 * k + 1] = v4u{phi(src[0][k], src[1][k]), phi(src[2][k], src[3][k]), phi(src[4][k], src[5][k]), phi(src[6][k], src[7][k])};
    PIN(wc[2 * k]); PIN(wc[2 * k + 1]);
  }
}
template <bool TWO> __device__ __forceinline__ v4f tile_gemv(const v4u (&w0)[8], v4u in0, const v4u (&w1)[8], v4u in1, int lane) {
  v4f d[8], r = {0.f, 0.f, 0.f, 0.f};
#pragma unroll
  for (int c = 0; c < 10; ++c) {
    if (c < 8) { d[c] = mfma(w0[c], in0, v4f{0.f, 0.f, 0.f, 0.f}); if (TWO) d[c] = mfma(w1[c], in1, d[c]); }
    if (c >= 2) { const bool m = (lane & 7) == c - 2; r = v4f{m ? d[c - 2].x : r.x, m ? d[c - 2].y : r.y, m ? d[c - 2].z : r.z, m ? d[c - 2].w : r.w}; }
    SB();
  }
  return r;
}
__device__ __forceinline__ void publish_tile(rsrc_t ws, v4f y, int so, int so2, unsigned tag, int lane) {
  const bool hi = lane & 8;
  const float r0 = dpp<ROR8>(hi ? y.x : y.y), r1 = dpp<ROR8>(hi ? y.z : y.w);
  __builtin_amdgcn_raw_buffer_store_b128(v4u{__float_as_uint(hi ? r0 : y.x), tag, __float_as_uint(hi ? y.y : r0), tag}, ws, so, 0, AUX_SC1);
  __builtin_amdgcn_raw_buffer_store_b128(v4u{__float_as_uint(hi ? r1 : y.z), tag, __float_as_uint(hi ? y.w : r1), tag}, ws, so2, 0, AUX_SC1);
}

struct Params {
  const u16 *x, *K, *V, *wq, *wo, *wg, *wu, *wd, *c, *wmod;
  u16* out;
  unsigned char* ws;
};

#define STAMP(i) do { } while (0)
#define DIAG_INIT() do { } while (0)
#define DIAG_FLUSH() do { } while (0)

constexpr int issued_before(int n) {
  int a = 32 + NVL + 1 + (n < 32 ? n : 31) / QDIV, b = n + WIN;
  int m = a > b ? a : b;
  return m > 64 ? 64 : m;
}
__device__ __forceinline__ void lds_spin_ge(volatile LAS unsigned* word, unsigned want) {
#pragma unroll 1
  for (unsigned spins = 0; spins < (SPIN_CAP << 5); ++spins) { if (*word >= want) break; }
  asm volatile("" ::: "memory");
  SB();
}
__device__ __forceinline__ void wait_until(unsigned due) {
  while ((int)((unsigned)__builtin_amdgcn_s_memrealtime() - due) < 0) __builtin_amdgcn_s_sleep(1);
  SB();
}
__device__ __forceinline__ void lds_inc(LAS unsigned char* L, int word, int lane) {
  ldswait();
  if (lane == 0) __hip_atomic_fetch_add((LAS unsigned*)(L + L_FLAG + word * 4), 1u, __ATOMIC_RELAXED, __HIP_MEMORY_SCOPE_WORKGROUP);
  SB();
}
constexpr v4f Z4 = {0.f, 0.f, 0.f, 0.f};

extern "C" __global__ void __launch_bounds__(NT, 3) mk_kernel(Params p) {
  extern __shared__ __attribute__((aligned(16))) unsigned char smem[];
  const u64 tstart = __builtin_amdgcn_s_memrealtime();
  asm volatile("" :: "s"(tstart));
  SB();
  LAS unsigned char* L = (LAS unsigned char*)smem;
  const int bid = blockIdx.x, t = threadIdx.x, lane = t & 63, w = __builtin_amdgcn_readfirstlane(t >> 6);
  const int x = bid & 7, j = bid >> 3;
  const int hkv = x, b = j >> 4, s = j & 15;
  const int lo16 = lane * 16, g4 = lane >> 4, n16 = lane & 15, hi3 = lane >> 3, lo3 = lane & 7;
  volatile LAS unsigned* flag = (volatile LAS unsigned*)(L + L_FLAG);
  const bool roleF = (j >= 13 && j < 16) || (j == 12 && x >= 4);
  const int f = j >= 13 ? (j - 13) * 8 + x : 20 + x;
  DIAG_INIT();
  STAMP(0);

  if (w < 4) {
    __builtin_amdgcn_s_setprio(2);
    const unsigned th0 = (unsigned)__builtin_amdgcn_s_memrealtime();
    const rsrc_t ws = mkrsrc(p.ws, WS_BYTES);
    const bool roleB = j < 8;
    const bool roleA = (j >= 8 && j < 12) || (j == 12 && x < 3);
    const bool roleS = j >= 16;
    const int e = x * 16 + s;
    const bool on = t < D / 2;
    LAS unsigned* H = (LAS unsigned*)(L + L_H + t * 4);
    const int unit = roleB ? x * 8 + j : x + 8 * (j - 8);
    const int vd = (w * 16 + n16 < NCG) ? g4 * 8 * D * 2 + w * 256 + n16 * 16 : OOB;
    const int pitch = roleB ? HQ * DH * 2 : roleA ? 6 * D * 2 : D * 2;
    const rsrc_t rw = mkrsrc(roleB ? p.wq : roleA ? p.wmod : roleF ? p.wd + (size_t)f * 64 * D : p.wo + (size_t)e * 32 * D, ((roleA | roleB) ? D : roleF ? 64 : 32) * pitch);
    const int va = (128 * w + 8 * hi3) * pitch + unit * 128 + lo3 * 16;
    const int vo = va, vo2 = w < 3 ? va + 64 * pitch : OOB;
    const u16* gu = ((lane & 8) ? p.wu : p.wg) + (size_t)(8 * g4) * FF + (roleF ? f : 0) * 64 + lo3 * 8;
#define GU_PIECE(i_) { const int q = (i_) < 56 ? ((i_) >> 3) * 16 + ((i_) & 7) : (((i_) - 56) >> 3) * 16 + 8 + ((i_) & 7); const u16* src_ = gu + (size_t)(32 * ((q) >> 3) + ((q) & 7)) * FF; unsigned keep_; \
      asm volatile("s_mov_b32 %0, m0\n\ts_mov_b32 m0, %1\n\ts_nop 0\n\tglobal_load_lds_dwordx4 %2, off nt\n\ts_mov_b32 m0, %0" \
                   : "=&s"(keep_) : "s"((unsigned)(size_t)(L + L_W) + (q) * 1024), "v"(src_) : "memory"); }
    v4u wt[16], xr0 = {0u, 0u, 0u, 0u}, xr1 = xr0;
    unsigned xo0 = 0u, xo1 = 0u;
    if (roleA | roleB) {
      const rsrc_t rx = mkrsrc(roleB ? p.x : p.c, 2 * D * 2);
      const int xo = on ? t * 4 : OOB, xv = (roleB && lane < 56) ? lo16 : OOB;
#pragma unroll
      for (int r = 0; r < 8; ++r) wt[r] = ldnt(rw, vo, r * pitch);
      xo0 = __builtin_amdgcn_raw_buffer_load_b32(rx, xo, 0, 0); xo1 = __builtin_amdgcn_raw_buffer_load_b32(rx, xo, D * 2, 0);
      xr0 = __builtin_amdgcn_raw_buffer_load_b128(rx, xv, 0, 0); xr1 = __builtin_amdgcn_raw_buffer_load_b128(rx, xv, D * 2, 0);
#pragma unroll
      for (int r = 8; r < 16; ++r) wt[r] = ldnt(rw, vo2, (r & 7) * pitch);
      SB();
    } else if (roleF) {
#pragma unroll 1
      for (int n = 0; n < NGE; ++n) GU_PIECE(4 * n + w)
    }
    if (t < 8) flag[t] = 0u;
    wgbar();
    unsigned ep;
    asm volatile("s_load_dword %0, %1, 0x0\n\ts_waitcnt lgkmcnt(0)" : "=s"(ep) : "s"(p.ws) : "memory");
    const unsigned tag = ep + 1u ? ep + 1u : 1u;
    v4u wc0[8], wc1[8];
#define UNIT_GEMV(UNIT, ISA, CNT) { \
      ldswait(); \
      const LAS v4u* HA = (const LAS v4u*)(L + L_H + (lane & 1) * 1024 + (128 * w + 16 * g4 + (lane & 4) * 2) * 2); \
      const v4u ha0 = HA[0], ha1 = HA[8]; \
      v4f y[8]; \
_Pragma("unroll") \
      for (int c = 0; c < 8; ++c) y[c] = mfma(ha0, wc0[c], Z4); \
_Pragma("unroll") \
      for (int c = 0; c < 8; ++c) y[c] = mfma(ha1, wc1[c], y[c]); \
      if (lane < 8 || (lane >= 24 && lane < 32)) { \
        LAS v2f* G = (LAS v2f*)(L + L_W + w * 1024 + (lane & 8) * 64 + lo3 * 64); \
_Pragma("unroll") \
        for (int c = 0; c < 8; ++c) G[c] = v2f{y[c].x, y[c].y}; \
      } \
      lds_inc(L, 2, lane); \
      if (w == 0) { \
        lds_wait_ge(flag + 2, CNT); \
        const LAS v4f* G = (const LAS v4f*)(L + L_W + (lane >> 5) * 2048 + (lane & 31) * 16); \
        const v4f a = (G[0] + G[32]) + (G[64] + G[96]); \
        const float e0 = (ISA) ? tr32(a.x, a.z) : tr32(a.x, a.y), e1 = (ISA) ? tr32(a.y, a.w) : tr32(a.z, a.w); \
        const int so = (ISA) ? OFF_MOD + (64 * (UNIT) + 2 * (lane & 31) + (lane >> 5)) * 8 : OFF_Q + ((lane >> 5) * 2048 + (UNIT) * 32 + (lane & 31)) * 8; \
        stsc2(ws, so, v2u{pk2(e0, e1), tag}); \
        SB(); \
        STAMP(1); \
      } \
    }
    float n00 = 0.f, n01 = 0.f, n10 = 0.f, n11 = 0.f;
    if (roleA) {
      H[0] = pk2(silu(bflo(xo0)), silu(bfhi(xo0))); H[256] = pk2(silu(bflo(xo1)), silu(bfhi(xo1)));
    } else if (roleB) {
      float s0 = 0.f, s1 = 0.f;
#pragma unroll
      for (int k = 0; k < 4; ++k) { s0 += bflo(xr0[k]) + bfhi(xr0[k]); s1 += bflo(xr1[k]) + bfhi(xr1[k]); }
      const float m0 = wave_sum(s0) * (1.f / D), m1 = wave_sum(s1) * (1.f / D);
      float v0 = 0.f, v1 = 0.f;
#pragma unroll
      for (int k = 0; k < 4; ++k) {
        const float a = bflo(xr0[k]) - m0, c = bfhi(xr0[k]) - m0, d = bflo(xr1[k]) - m1, q = bfhi(xr1[k]) - m1;
        v0 += a * a + c * c; v1 += d * d + q * q;
      }
      if (lane >= 56) { v0 = 0.f; v1 = 0.f; }
      v0 = wave_sum(v0) * (1.f / D); v1 = wave_sum(v1) * (1.f / D);
      const float r0 = __builtin_amdgcn_rcpf(rbf(__builtin_amdgcn_sqrtf(rbf(rbf(v0) + 1e-5f)))), r1 = __builtin_amdgcn_rcpf(rbf(__builtin_amdgcn_sqrtf(rbf(rbf(v1) + 1e-5f))));
      const float mb0 = rbf(m0), mb1 = rbf(m1);
      n00 = rbf(rbf(bflo(xo0) - mb0) * r0); n01 = rbf(rbf(bfhi(xo0) - mb0) * r0); n10 = rbf(rbf(bflo(xo1) - mb1) * r1); n11 = rbf(rbf(bfhi(xo1) - mb1) * r1);
    }
    PIN(n00); PIN(n01); PIN(n10); PIN(n11);
    if (roleA | roleB) { tile_cols(wt, wc0); tile_cols(wt + 8, wc1); }
    __builtin_amdgcn_s_waitcnt(0x0F70);
    waitvm<0>();
    STAMP(19);
    const unsigned tland = (unsigned)__builtin_amdgcn_s_memrealtime() - ((roleA | roleB | roleF) ? TLANDH : TLANDL) / 10;
    const unsigned tref = (int)(tland - th0) > 0 ? tland : th0;
    if (roleB) {
      wait_until(tref + TMOD / 10);
      STAMP(26);
      int off[2]; v2u g[2];
      off[0] = on ? OFF_MOD + t * 16 : OOB; off[1] = on ? OFF_MOD + D * 8 + t * 16 : OOB;
      gather<2, PSLEEP>(ws, off, g, tag);
      STAMP(2);
      const unsigned c0 = pk2(1.f + bflo(g[1].x), 1.f + bflo(g[1].y)), c1 = pk2(1.f + bfhi(g[1].x), 1.f + bfhi(g[1].y));
      const unsigned u0 = pk2(n00 * bflo(c0), n01 * bfhi(c0)), u1 = pk2(n10 * bflo(c1), n11 * bfhi(c1));
      H[0] = on ? pk2(bflo(u0) + bflo(g[0].x), bfhi(u0) + bflo(g[0].y)) : 0u; H[256] = on ? pk2(bflo(u1) + bfhi(g[0].x), bfhi(u1) + bfhi(g[0].y)) : 0u;
    }
    if (roleA | roleB) UNIT_GEMV(unit, roleA, 4u)
    if (w == 1) {
      u64 pc; unsigned back, l1, l2, l3;
      asm volatile("s_getpc_b64 %0\n.Lmk_anchor:\n\ts_mov_b32 %1, .Lmk_anchor-mk_kernel\n\ts_mov_b32 %2, .Lmk_end1-mk_kernel\n\ts_mov_b32 %3, .Lmk_end2-mk_kernel\n\ts_mov_b32 %4, .Lmk_end4-mk_kernel"
                   : "=s"(pc), "=s"(back), "=s"(l1), "=s"(l2), "=s"(l3));
      const unsigned len = max(l1, max(l2, l3));
      const unsigned lines = (len + 127u) >> 7, per = (lines + 31u) >> 5, li = min(j * per + min((unsigned)lane, per - 1u), lines - 1u), co = li * 128u;
      const unsigned cpf = *(const GAS unsigned*)(pc - back + co);
      asm volatile("" :: "v"(cpf));
      if (CODEPF > 1) {
        lds_wait_ge(flag + 6, 1u);
        const unsigned cp2 = __hip_atomic_load((const GAS unsigned*)(pc - back + co), __ATOMIC_RELAXED, __HIP_MEMORY_SCOPE_AGENT);
        asm volatile("" :: "v"(cp2));
      }
    }
    if (w == 2 && j == 31 && x < 4) {
      __builtin_amdgcn_raw_buffer_store_b32(0u, ws, n16 < NPROBE ? OFF_PRB + (g4 & 1) * 8192 + (g4 >> 1) * (OFF_PRB1 - OFF_PRB) + x * 2048 + n16 * 128 : OOB, 0, AUX_SC1);
      waitvm<0>();
      unsigned mn[4] = {~0u, ~0u, ~0u, ~0u};
#pragma unroll 1
      for (int r = 0; r < NPROBE; ++r) {
        const int pb = __builtin_amdgcn_readfirstlane(OFF_PRB + x * 2048 + r * 128);
#pragma unroll
        for (int k = 0; k < 4; ++k) {
          u64 a, c; unsigned v;
          asm volatile("s_memtime %0\n\ts_waitcnt lgkmcnt(0)\n\ts_load_dword %2, %3, %4\n\ts_waitcnt lgkmcnt(0)\n\ts_memtime %1\n\ts_waitcnt lgkmcnt(0)"
                       : "=&s"(a), "=&s"(c), "=&s"(v) : "s"(p.ws), "s"(pb + (k & 1) * 8192 + (k >> 1) * (OFF_PRB1 - OFF_PRB)) : "memory");
          mn[k] = min(mn[k], (unsigned)(c - a));
        }
      }
      const unsigned bits = ((mn[1] < mn[0]) | (mn[3] < mn[2]) << 1) ^ FLIP;
      stsc(ws, lane == 0 ? OFF_PREF + x * 128 : OOB, v4u{bits, tag, bits, tag});
      STAMP(21);
    }
    if (TLBW && w == 2) {
      lds_wait_ge(flag + 3, 1u);
      const unsigned char* a = (lane == 1 && roleS) ? (const unsigned char*)(p.wo + (size_t)e * 32 * D) : (lane == 2 && roleF) ? (const unsigned char*)(p.wd + (size_t)f * 64 * D)
                             : p.ws + WS_BYTES + bid * 128;
      const unsigned v = __builtin_nontemporal_load((const GAS unsigned*)a);
      asm volatile("" :: "v"(v));
    }
    if (w == 3) {
      wait_until(tref + TQ / 10);
      STAMP(25);
      {
        int off[2]; v2u g[2];
        off[0] = OFF_Q + (b * 2048 + hkv * 256) * 8 + lo16; off[1] = off[0] + 1024;
        gather<2, PSLEEP>(ws, off, g, tag);
        *(LAS v2u*)(L + L_Q + lane * 8) = v2u{g[0].x, g[0].y}; *(LAS v2u*)(L + L_Q + 512 + lane * 8) = v2u{g[1].x, g[1].y};
        if (lane == 0) flag[1] = tag;
        lds_inc(L, 0, lane);
      }
      STAMP(4);
      lds_wait_ge(flag + 3, 8u);
      const int h = g4 & 1;
      const v2f ml = *(const LAS v2f*)(L + L_ML + (h * 8 + lo3) * 8);
      float M = ml.x;
      M = fmaxf(M, dpp<XOR1>(M)); M = fmaxf(M, dpp<XOR2>(M)); M = fmaxf(M, dpp<XOR7>(M));
      float Ls = __builtin_amdgcn_exp2f(ml.x - M) * ml.y;
      Ls += dpp<XOR1>(Ls); Ls += dpp<XOR2>(Ls); Ls += dpp<XOR7>(Ls);
      const int mo = OFF_ML + (b * 8 + hkv) * 2048 + h * 16;
      stsc(ws, (lane == 0 || lane == 16) ? mo + s * 128 : OOB, v4u{__float_as_uint(M), tag, __float_as_uint(Ls), tag});
      waitvm<0>();
      lds_wait_ge(flag + 6, 1u);
      int off[2]; v2u g[2];
      off[0] = mo + n16 * 128; off[1] = lane < 16 ? OFF_PREF + (lane & 3) * 128 : OOB;
      gather<2, PSLEEP>(ws, off, g, tag);
      const unsigned nb = (unsigned)__ballot(((g[1].x >> (lane >> 3)) ^ (lane >> 2)) & 1) & 0xffffu;
      if (lane == 0) flag[8] = nb;
      float G = __uint_as_float(g[0].x);
      G = fmaxf(G, dpp<XOR1>(G)); G = fmaxf(G, dpp<XOR2>(G)); G = fmaxf(G, dpp<ROR4>(G)); G = fmaxf(G, dpp<ROR8>(G));
      float T = __builtin_amdgcn_exp2f(__uint_as_float(g[0].x) - G) * __uint_as_float(g[0].y);
      T += dpp<XOR1>(T); T += dpp<XOR2>(T); T += dpp<ROR4>(T); T += dpp<ROR8>(T);
      if (lane < 32 && !(lane & 8)) *(LAS float*)(L + L_FW + (h * 8 + lo3) * 4) = __builtin_amdgcn_exp2f(ml.x - G) / T;
      lds_inc(L, 5, lane);
      STAMP(3);
    }
    v2u gm[2] = {{0u, 0u}, {0u, 0u}};
    const int cg = j * 8 + x;
    unsigned xin0 = 0, xin1 = 0;
    if (j < 7 && w == 0) {
      lds_wait_ge(flag + 6, 1u);
      xin0 = p.x[8 * cg + lo3]; xin1 = p.x[D + 8 * cg + lo3];
      int om[2]; om[0] = OFF_MOD + (2 * D + 8 * cg + lo3) * 8; om[1] = OOB;
      gather2<2>(ws, om, gm, tag);
      PIN(xin0); PIN(xin1);
    }
    lds_wait_ge(flag + 5, 1u);
    const unsigned near = __builtin_amdgcn_readfirstlane(flag[8]);
    if (roleS | roleF) {
      waitvm<0>();
      lds_wait_ge(flag + 4, TAILN);
      if (roleS) {
#pragma unroll
        for (int r = 0; r < 8; ++r) wt[r] = ldnt(rw, vd, r * pitch);
        SB();
      } else {
#pragma unroll 1
        for (int n = NGE; n < NGE + NGT; ++n) GU_PIECE(4 * n + w)
      }
    }
    {
      const int h = w >> 1, dp = t & 127;
      v4f f0 = *(const LAS v4f*)(L + L_FW + h * 32), f1 = *(const LAS v4f*)(L + L_FW + h * 32 + 16);
      int po = po_base((2 * hkv + h) * 8 + (dp >> 4), near) + (b * 16 + s) * 256 + (dp & 15) * 16;
      PIN(f0); PIN(f1); PIN(po);
      wgbar();
      const LAS v2f* O = (const LAS v2f*)(L + L_V + h * 1024 + dp * 8);
      v2f v[8];
#pragma unroll
      for (int k = 0; k < 8; ++k) v[k] = O[k * 512];
      SB();
      const v2f o = ((v[0] * f0.x + v[1] * f0.y) + (v[2] * f0.z + v[3] * f0.w)) + ((v[4] * f1.x + v[5] * f1.y) + (v[6] * f1.z + v[7] * f1.w));
      stsc(ws, po, v4u{__float_as_uint(o.x), tag, __float_as_uint(o.y), tag});
      SB();
    }
    STAMP(9);
    const int cg0 = 16 * w + 4 * g4;

    if (roleS) {
      tile_cols(wt, wc0);
      STAMP(20);
      int off[2]; v2u g[2];
      off[0] = po_base(e, near) + (w >> 1) * 4096 + hi3 * 256 + (w & 1) * 128 + lo3 * 16; off[1] = off[0] + 2048;
      int so = cg0 < NCG ? y1_line(cg0 + (hi3 & 1), e, near) + lo3 * 16 : OOB, so2 = cg0 < NCG ? y1_line(cg0 + 2 + (hi3 & 1), e, near) + lo3 * 16 : OOB;
      PIN(so); PIN(so2);
      if (HOLD1) wait_until(th0 + (TEND + HOLD1) / 10);
      if (GALL & 1) gather_all<2>(ws, off, g, tag); else gather<2>(ws, off, g, tag);
      STAMP(10);
      float v = tr32(__uint_as_float(g[0].x) + __uint_as_float(g[1].x), __uint_as_float(g[0].y) + __uint_as_float(g[1].y));
      v = tr16(v, v); v += dpp<ROR8>(v);
      if ((lane & 24) == 0) *(LAS u16*)(L + L_S1 + w * 32 + lo3 * 4 + (lane >> 5) * 2) = (u16)pk2(v, v);
      wgbar();
      const v4u ob = *(const LAS v4u*)(L + L_S1 + (lane & 8) * 8 + g4 * 16);
      publish_tile(ws, tile_gemv<false>(wc0, ob, wc0, ob, lane), so, so2, tag, lane);
      STAMP(11);
      asm volatile(".Lmk_end1:");
    } else if (roleF) {
#pragma unroll 1
      for (int n = NGE + NGT; n < 28; ++n) GU_PIECE(4 * n + w)
#pragma unroll
      for (int r = 0; r < 16; ++r) wt[r] = ldnt(rw, vd, ((r >> 3) * 32 + (r & 7)) * pitch);
      SB();
      waitvm<16>();
      STAMP(14);
      wgbar();
      int so = cg0 < NCG ? y2_line(cg0 + (hi3 & 1), f, near) + lo3 * 16 : OOB, so2 = cg0 < NCG ? y2_line(cg0 + 2 + (hi3 & 1), f, near) + lo3 * 16 : OOB;
      PIN(so); PIN(so2);
      tile_cols(wt, wc0); tile_cols(wt + 8, wc1);
      STAMP(20);
      lds_wait_ge(flag + 2, 7u);
      v4f cg_ = Z4, cu_ = Z4;
#pragma unroll
      for (int k = 0; k < 7; ++k) { cg_ += *(const LAS v4f*)(L + L_V + k * 4096 + 1024 + lane * 32); cu_ += *(const LAS v4f*)(L + L_V + k * 4096 + 1024 + lane * 32 + 16); }
      PIN(cg_); PIN(cu_);
      lds_spin_ge(flag + 7, 7u);
      const v4f a0 = *(const LAS v4f*)(L + L_ST + (lane & 8) * 16), ak = *(const LAS v4f*)(L + L_ST + n16 * 16);
      const float ek = ak.x - a0.x;
      float q1 = ak.y + 64.f * ek, q2 = ak.z + ek * (ak.y + q1);
      if (lo3 == 7) { q1 = 0.f; q2 = 0.f; }
      q1 += dpp<XOR1>(q1); q2 += dpp<XOR1>(q2); q1 += dpp<XOR2>(q1); q2 += dpp<XOR2>(q2); q1 += dpp<XOR7>(q1); q2 += dpp<XOR7>(q2);
      const float m1 = q1 * (1.f / D);
      const float mean = rbf(a0.x + m1), rs = __builtin_amdgcn_rcpf(rbf(__builtin_amdgcn_sqrtf(rbf(rbf(fmaxf(q2 * (1.f / D) - m1 * m1, 0.f)) + 1e-5f))));
      const float mo = dpp<ROR8>(mean), ro = dpp<ROR8>(rs);
      const bool hi = lane & 8;
      float r0 = hi ? ro : rs, r1 = hi ? rs : ro;
      const float mr0 = (hi ? mo : mean) * r0, mr1 = (hi ? mean : mo) * r1;
      v4f off4 = {cg_.z - mr0 * cg_.x, cg_.w - mr1 * cg_.y, cu_.z - mr0 * cu_.x, cu_.w - mr1 * cu_.y};
      PIN(off4); PIN(r0); PIN(r1);
      wgbar();
      v4f gw[7];
#pragma unroll
      for (int k = 0; k < 7; ++k) gw[k] = *(const LAS v4f*)(L + L_GU + k * 1024 + lo16);
      SB();
      const v4f a = (((gw[0] + gw[1]) + (gw[2] + gw[3])) + ((gw[4] + gw[5]) + gw[6])) * v4f{r0, r1, r0, r1} + off4;
      const unsigned gb = pk2(a.x, a.y), ub = pk2(a.z, a.w), sg = pk2(silu(bflo(gb)), silu(bfhi(gb)));
      const unsigned ac = pk2(bflo(sg) * bflo(ub), bfhi(sg) * bfhi(ub));
      *(LAS u16*)(L + L_ACT + w * 256 + lo3 * 16 + hi3 * 2) = (u16)ac; *(LAS u16*)(L + L_ACT + w * 256 + 128 + lo3 * 16 + hi3 * 2) = (u16)(ac >> 16);
      asm volatile("" ::: "memory");
      const LAS v4u* E = (const LAS v4u*)(L + L_ACT + w * 256 + (lane & 8) * 16 + g4 * 16);
      const v4u e0 = E[0], e1 = E[4];
      publish_tile(ws, tile_gemv<true>(wc0, e0, wc1, e1, lane), so, so2, tag, lane);
      STAMP(16);
      asm volatile(".Lmk_end2:");
    } else if (j < 7) {
      int of[4]; v2u gg[4];
#pragma unroll
      for (int k = 0; k < 4; ++k) of[k] = y1_line(cg, k * 32, near) + t * 16;
      if (HOLD2) wait_until(th0 + (TEND + HOLD2) / 10);
      if (GALL & 2) gather_all<4>(ws, of, gg, tag); else gather<4>(ws, of, gg, tag);
      STAMP(12);
      const float a0 = (__uint_as_float(gg[0].x) + __uint_as_float(gg[1].x)) + (__uint_as_float(gg[2].x) + __uint_as_float(gg[3].x));
      const float a1 = (__uint_as_float(gg[0].y) + __uint_as_float(gg[1].y)) + (__uint_as_float(gg[2].y) + __uint_as_float(gg[3].y));
      float v2 = tr32(a0, a1); v2 = tr16(v2, v2); v2 += dpp<ROR8>(v2);
      if ((lane & 24) == 0) *(LAS float*)(L + L_RED + (w * 16 + lo3 * 2 + (lane >> 5)) * 4) = v2;
      wgbar();
      if (w == 0) {
        const LAS v2f* R = (const LAS v2f*)(L + L_RED + lo3 * 8);
        const v2f y1 = (R[0] + R[8]) + (R[16] + R[24]);
        const unsigned yb = pk2(y1.x, y1.y), gy = pk2(bflo(gm[0].x) * bflo(yb), bfhi(gm[0].x) * bfhi(yb));
        const unsigned x1v = pk2(bflo(xin0) + bflo(gy), bflo(xin1) + bfhi(gy));
        const unsigned xn = __builtin_amdgcn_mov_dpp(x1v, XOR1, 0xf, 0xf, true);
        stsc2(ws, lane < 16 ? OFF_X1 + (lane & 8) * 1024 + cg * 128 + (lane & 1) * 32 + ((lane >> 1) & 3) * 8 : OOB, v2u{(lane & 1) ? phi(xn, x1v) : plo(x1v, xn), tag});
        SB();
        STAMP(13);
        int og[1]; v2u gv[1];
        og[0] = OFF_MOD + (5 * D + 8 * cg + (lo3 & 6)) * 8;
        gather<1, 4>(ws, og, gv, tag);
        unsigned gbv = (lane & 1) ? gv[0].y : gv[0].x;
        PIN(gbv);
        int o2[4]; v2u g2[4];
#pragma unroll
        for (int k = 0; k < 4; ++k) o2[k] = (k * 8 + hi3 < N2) ? y2_line(cg, 0, near) + k * 1024 + lo16 : OOB;
        if (GALL & 4) gather_all<4>(ws, o2, g2, tag, lane >= 32); else gather<4>(ws, o2, g2, tag);
        STAMP(17);
        const float c0 = (__uint_as_float(g2[0].x) + __uint_as_float(g2[1].x)) + (__uint_as_float(g2[2].x) + __uint_as_float(g2[3].x));
        const float c1 = (__uint_as_float(g2[0].y) + __uint_as_float(g2[1].y)) + (__uint_as_float(g2[2].y) + __uint_as_float(g2[3].y));
        float y2 = tr32(c0, c1); y2 = tr16(y2, y2); y2 += dpp<ROR8>(y2);
        const bool hb = lane >= 32;
        const float res = (hb ? bfhi(x1v) : bflo(x1v)) + rbf((hb ? bfhi(gbv) : bflo(gbv)) * rbf(y2));
        if ((__builtin_amdgcn_s_getreg(20 | (3 << 11)) & 0xfu) == 4) while (__builtin_amdgcn_s_memrealtime() - tstart < 5000) { }
        if ((lane & 24) == 0) p.out[(lane >> 5) * D + 8 * cg + lo3] = (u16)pk2(res, res);
        if (cg == 0 && lane == 0) __hip_atomic_store((GAS unsigned*)(p.ws + OFF_EPOCH), tag, __ATOMIC_RELAXED, __HIP_MEMORY_SCOPE_AGENT);
        STAMP(18);
        asm volatile(".Lmk_end3:");
      }
    }
    if (j == 8 && x < 7) {
      const int v1 = (128 * w + 8 * hi3) * pitch + (35 + x) * 128 + lo3 * 16, v2 = w < 3 ? v1 + 64 * pitch : OOB;
#pragma unroll
      for (int r = 0; r < 16; ++r) wt[r] = ldnt(rw, r < 8 ? v1 : v2, (r & 7) * pitch);
      SB();
      tile_cols(wt, wc0); tile_cols(wt + 8, wc1);
      UNIT_GEMV(35 + x, true, 8u)
      asm volatile(".Lmk_end4:");
    }
  } else {
    const int sw = w - 4;
    const size_t kvoff = ((size_t)(b * HKV + hkv) * SK + s * 512 + sw * 64) * DH;
    const rsrc_t rK = mkrsrc(p.K + kvoff, 64 * DH * 2), rV = mkrsrc(p.V + kvoff, 64 * DH * 2);
    const int vk = lo3 * 512 + (lane & 8) * 8 + g4 * 16;
    const int vv = g4 * 512 + n16 * 16;
    v4u kv[64];
    const bool heavy = j < 16 && !(j == 12 && x == 3);
    const unsigned tau = heavy ? (TEND - P0H) * 16 / 630 : (TEND - P0L) * 16 / 630;
    const unsigned tauf = tau * 100 / (100 + CATCH);
    const unsigned ts0 = (unsigned)__builtin_amdgcn_s_memrealtime();
    unsigned due = ts0 << 4, lim;
#define PACE(i) { due += tau; lim += tauf; unsigned now_ = (unsigned)__builtin_amdgcn_s_memrealtime() << 4; \
      if ((int)(now_ - BURST * tauf - lim) > 0) lim = now_ - BURST * tauf; \
      if ((int)(due - lim) > 0) lim = due; \
      while ((int)(now_ - lim) < 0) now_ = (unsigned)__builtin_amdgcn_s_memrealtime() << 4; }
    const int rot = ROT ? sw + j : 0;
#define KSEG(st) ((((st) + rot) & 3) * 128)
#define VSEG(i) ((((i) + rot) & 1) * 256)
#define LOADKV(i) kv[i] = (i) < 32 ? ldnt(rK, vk, ((i) >> 2) * 4096 + KSEG(i)) : ldnt(rV, vv, (((i) - 32) >> 1) * 2048 + VSEG(i))
#define ISSUE_TO(FROM, TO) _Pragma("unroll") for (int i_ = (FROM); i_ < (TO); ++i_) { if (i_ >= FIRST) PACE(i_) LOADKV(i_); SB(); }
    ISSUE_TO(0, FIRST)
    STAMP(22);
    if (BAR_AT < FIRST) wgbar();
    STAMP(23);
    if (ANCHOR) {
      waitvm<FIRST - 1>();
      const unsigned a = ((unsigned)__builtin_amdgcn_s_memrealtime() << 4) - ANCHOR * 16 / 10;
      if ((int)(a - due) > 0) due = a;
    }
    due += (heavy ? P0H : P0L) * 16 / 10 + (FIRST - 1) * tau + sw * (tau >> 3); lim = due;
#pragma unroll
    for (int i = FIRST; i < 32; ++i) {
      waitvm<TD - 1>(); PACE(i) LOADKV(i); SB();
      if (i == BAR_AT) wgbar();
    }
    {
      const unsigned lb = (unsigned)(size_t)(L + L_V) + sw * 4096;
#pragma unroll
      for (int i = 0; i < NVL; ++i) {
        waitvm<TD - 1>();
        PACE(32 + i)
        unsigned keep;
        asm volatile("s_mov_b32 %0, m0\n\ts_mov_b32 m0, %1\n\ts_nop 0\n\tbuffer_load_dwordx4 %2, %3, %4 offen nt lds\n\ts_mov_b32 m0, %0"
                     : "=&s"(keep) : "s"(lb + i * 1024), "v"(vv), "s"(rV), "s"((i >> 1) * 2048 + VSEG(i)) : "memory");
        SB();
      }
    }
    STAMP(5);
    lds_wait_ge(flag, 1u);
    v4u qa[4];
#pragma unroll
    for (int st = 0; st < 4; ++st) qa[st] = *(const LAS v4u*)(L + L_Q + (lane & 1) * 512 + KSEG(st) + (lane & 2) * 32 + g4 * 16);
    STAMP(6);
    float ev0 = 0.f, ev1 = 0.f, ov0 = 0.f, ov1 = 0.f;
    const bool c2 = lane & 8;
#pragma unroll
    for (int tt = 0; tt < 8; ++tt) {
      v4f a = Z4;
#pragma unroll
      for (int st = 0; st < 4; ++st) {
        const int n = tt * 4 + st;
        ISSUE_TO(n == 0 ? 32 + NVL : issued_before(n - 1), issued_before(n))
        a = mfma(qa[st], kv[n], a);
        SB();
      }
      const float v0 = c2 ? a.z : a.x, v1 = c2 ? a.w : a.y;
      const bool mine = g4 == (tt >> 1);
      if (tt & 1) { ov0 = mine ? v0 : ov0; ov1 = mine ? v1 : ov1; } else { ev0 = mine ? v0 : ev0; ev1 = mine ? v1 : ev1; }
    }
    const float sc0 = rbf(trd<ROR8>(ev0, ov0, c2)) * (0.0625f * 1.4426950408889634f);
    const float sc1 = rbf(trd<ROR8>(ev1, ov1, c2)) * (0.0625f * 1.4426950408889634f);
    {
      auto sx = __builtin_amdgcn_permlane32_swap(__float_as_uint(sc0), __float_as_uint(sc1), false, false);
      float m = fmaxf(__uint_as_float(sx[0]), __uint_as_float(sx[1]));
      m = max16(m); m = fmaxf(m, dpp<ROR8>(m)); m = fmaxf(m, dpp<ROR4>(m)); m = fmaxf(m, dpp<XOR2>(m)); m = fmaxf(m, dpp<XOR1>(m));
      const float m0 = __builtin_bit_cast(float, __builtin_amdgcn_readlane(__builtin_bit_cast(int, m), 0));
      const float m1 = __builtin_bit_cast(float, __builtin_amdgcn_readlane(__builtin_bit_cast(int, m), 32));
      const float p0 = __builtin_amdgcn_exp2f(sc0 - m0), p1 = __builtin_amdgcn_exp2f(sc1 - m1);
      float l = tr32(p0, p1);
      l = tr16(l, l); l += dpp<ROR8>(l); l += dpp<ROR4>(l); l += dpp<XOR2>(l); l += dpp<XOR1>(l);
      const unsigned pp = pk2(p0, p1);
      LAS unsigned* P = (LAS unsigned*)(L + L_P + sw * 1024 + (lane & 3) * 64 + (lane >> 2) * 4);
      P[0] = pp & 0xffffu; P[64] = pp << 16; P[128] = pp >> 16; P[192] = pp & 0xffff0000u;
      if ((lane & 31) == 0) *(LAS v2f*)(L + L_ML + ((lane >> 5) * 8 + sw) * 8) = v2f{m, l};
    }
    lds_inc(L, 3, lane);
    waitvm<issued_before(31) - 32 - NVL>();
#pragma unroll
    for (int i = 0; i < NVL; ++i) kv[32 + i] = *(const LAS v4u*)(L + L_V + sw * 4096 + i * 1024 + lo16);
    v4u pr[4];
#pragma unroll
    for (int k = 0; k < 4; ++k) pr[k] = *(const LAS v4u*)(L + L_P + sw * 1024 + (lane & 8) * 64 + (lane & 1) * 256 + g4 * 64 + k * 16);
    STAMP(7);
    const rsrc_t ws = mkrsrc(p.ws, WS_BYTES);
    int om[2];
    om[0] = (roleF && sw < 7) ? OFF_MOD + (3 * D + 64 * sw + 8 * hi3 + 2 * (lane & 3)) * 8 : OOB; om[1] = (roleF && sw < 7) ? om[0] + D * 8 : OOB;
    const v4u ms = ldsc(ws, om[0]), mc = ldsc(ws, om[1]);
    SB();
    v4f acc0 = Z4, acc1 = Z4;
    const int jq = (lane >> 1) & 3;
#pragma unroll
    for (int ip = 0; ip < 16; ++ip) {
      ISSUE_TO(issued_before(32 + 2 * ip - 1), issued_before(32 + 2 * ip + 1))
      if (ip == ML_AT) lds_inc(L, 6, lane);
      if (issued_before(32 + 2 * ip - 1) < 64 && issued_before(32 + 2 * ip + 1) == 64) lds_inc(L, 4, lane);
      const unsigned pu = pr[ip >> 2][ip & 3];
      const v4u pa = {jq == 0 ? pu : 0u, jq == 1 ? pu : 0u, jq == 2 ? pu : 0u, jq == 3 ? pu : 0u};
      acc0 = mfma(pa, kv[32 + 2 * ip], acc0); acc1 = mfma(pa, kv[33 + 2 * ip], acc1);
      SB();
    }
    LAS v4f* O = (LAS v4f*)(L + L_V + sw * 4096 + (g4 >> 1) * 1024 + n16 * 32 + (g4 & 1) * 16);
    O[VSEG(0) / 8] = acc0; O[VSEG(1) / 8] = acc1;
    STAMP(8);
    wgbar();
    if (roleF && sw < 7) {
      const unsigned tag = __builtin_amdgcn_readfirstlane(flag[1]);
      const bool hb = lane & 4;
      v2u gm[2] = {{ms.x, ms.z}, {mc.x, mc.z}};
      if (!__all((ms.y == tag) & (ms.w == tag) & (mc.y == tag) & (mc.w == tag))) gather<2>(ws, om, gm, tag);
      const unsigned cbp = hb ? phi(gm[1].x, gm[1].y) : plo(gm[1].x, gm[1].y), cb1 = pk2(1.f + bflo(cbp), 1.f + bfhi(cbp));
      LAS unsigned* Z = (LAS unsigned*)(L + L_V + sw * 4096 + 3072 + (lane & 4) * 32 + hi3 * 16 + (lane & 3) * 4);
      const LAS v4u* ZA = (const LAS v4u*)(L + L_V + sw * 4096 + 3072 + (lane & 3) * 128 + g4 * 16);
      Z[0] = cb1; Z[64] = hb ? phi(gm[0].x, gm[0].y) : plo(gm[0].x, gm[0].y);
      v4u wt[8], gc0[8], gc1[8];
      v4f y[8];
#pragma unroll
      for (int k = 0; k < 8; ++k) wt[k] = *(const LAS v4u*)(L + L_W + (sw * 16 + k) * 1024 + lo16);
      tile_cols(wt, gc0);
      {
        const v4u h0 = ZA[0], h1 = ZA[4];
#pragma unroll
        for (int c = 0; c < 8; ++c) { y[c] = mfma(h0, gc0[c], Z4); PIN(y[c]); }
        wgbar();
#pragma unroll
        for (int k = 0; k < 8; ++k) wt[k] = *(const LAS v4u*)(L + L_W + (sw * 16 + 8 + k) * 1024 + lo16);
        tile_cols(wt, gc1);
#pragma unroll
        for (int c = 0; c < 8; ++c) y[c] = mfma(h1, gc1[c], y[c]);
        if (lane < 16) {
          LAS v4f* G = (LAS v4f*)(L + L_V + sw * 4096 + 1024 + lo3 * 32 + (lane & 8) * 2);
#pragma unroll
          for (int c = 0; c < 8; ++c) G[16 * c] = y[c];
        }
      }
      float cl = bflo(cb1), ch = bfhi(cb1);
      int ox[1]; v2u gx[1];
      ox[0] = OFF_X1 + ((__builtin_amdgcn_readfirstlane(flag[8]) >> x) & 1) * 8192 + (8 * sw + hi3) * 128 + lo3 * 8;
      PIN(cl); PIN(ch); PIN(ox[0]);
      lds_inc(L, 2, lane);
      STAMP(28);
      if (HOLD3) wait_until(ts0 + (TEND + HOLD3) / 10);
      gather2<1>(ws, ox, gx, tag);
      STAMP(15);
      const float x0 = bflo(gx[0].x), x1 = bfhi(gx[0].x), z0 = x0 * cl, z1 = x1 * ch;
      const unsigned zh = pk2(z0, z1);
      Z[0] = zh; Z[64] = pk2(z0 - bflo(zh), z1 - bfhi(zh));
      asm volatile("" ::: "memory");
      const v4u h0 = ZA[0], h1 = ZA[4];
      SB();
      const unsigned pv0 = __builtin_amdgcn_readlane(gx[0].x, 0), pv1 = __builtin_amdgcn_readlane(gx[0].x, 4);
      const float pv = bflo(hb ? pv1 : pv0), d0 = x0 - pv, d1 = x1 - pv;
      float u = tr32(d0 + d1, d0 * d0 + d1 * d1);
      u = tr16(u, u); u += dpp<ROR8>(u); u += dpp<XOR1>(u); u += dpp<XOR2>(u);
      if ((lane & 26) == 0 && lane != 33 && lane != 37) *(LAS float*)(L + L_ST + (lane & 4) * 32 + sw * 16 + ((lane & 1) ? 0 : 4 + (lane >> 5) * 4)) = (lane & 1) ? pv : u;
      lds_inc(L, 7, lane);
#pragma unroll
      for (int c = 0; c < 8; ++c) y[c] = mfma(h1, gc1[c], mfma(h0, gc0[c], Z4));
      if (lane < 16) {
        LAS v2f* G = (LAS v2f*)(L + L_GU + sw * 1024 + lo3 * 16 + (lane & 8));
#pragma unroll
        for (int c = 0; c < 8; ++c) G[16 * c] = v2f{y[c].x + y[c].z, y[c].y + y[c].w};
      }
      wgbar();
    }
  }
  DIAG_FLUSH();
}

extern "C" void kernel_launch(void* const* d_in, const int* in_sizes, int n_in, void* d_out, int out_size, void* d_ws, size_t ws_size, hipStream_t stream) {
  Params p;
  p.x = (const u16*)d_in[0]; p.K = (const u16*)d_in[1]; p.V = (const u16*)d_in[2]; p.wq = (const u16*)d_in[3]; p.wo = (const u16*)d_in[4];
  p.wg = (const u16*)d_in[6]; p.wu = (const u16*)d_in[7]; p.wd = (const u16*)d_in[8]; p.c = (const u16*)d_in[9]; p.wmod = (const u16*)d_in[10];
  p.out = (u16*)d_out; p.ws = (unsigned char*)(((uintptr_t)d_ws + (2u << 20) - 1) & ~(uintptr_t)((2u << 20) - 1));
  (void)hipFuncSetAttribute((const void*)mk_kernel, hipFuncAttributeMaxDynamicSharedMemorySize, LDS_BYTES);
  hipLaunchKernelGGL(mk_kernel, dim3(NWG), dim3(NT), LDS_BYTES, stream, p);
}
```

```cpp
#define PX 1
#include <hip/hip_runtime.h>
#include <hip/hip_bf16.h>
#include <cstdint>

#define DIAG 0

typedef unsigned short u16;
typedef unsigned long long u64;
typedef unsigned v4u __attribute__((ext_vector_type(4)));
typedef unsigned v2u __attribute__((ext_vector_type(2)));
typedef float v4f __attribute__((ext_vector_type(4)));
typedef float v2f __attribute__((ext_vector_type(2)));
typedef __bf16 bf16x2 __attribute__((ext_vector_type(2)));
#define LAS __attribute__((address_space(3)))
#define GAS __attribute__((address_space(1)))
typedef __amdgpu_buffer_rsrc_t rsrc_t;

constexpr int D = 448, HQ = 16, HKV = 8, SK = 8192, DH = 256, FF = 1792;
constexpr int NWG = 256, NT = 768;
constexpr int N1 = 128;
constexpr int N2 = 28;
constexpr int NCG = 56;
constexpr int OFF_EPOCH = 0;
constexpr int OFF_PREF = 512;
constexpr int OFF_MOD = 1024;
constexpr int OFF_Q = 24576;
constexpr int OFF_T0 = 57344;
constexpr int OFF_SH = 59392;
constexpr int OFF_ML = 65536;
constexpr int OFF_PRB = 98304;
constexpr int OFF_X1 = 114688;
constexpr int OFF_Y1 = 131072;
constexpr int OFF_Y2 = OFF_Y1 + 56 * 16384;
constexpr int OFF_PO = 2 << 20;
constexpr int OFF_PRB1 = OFF_PO + 64 * 16384;
constexpr int WS_BYTES = OFF_PRB1 + 16384;
static_assert(OFF_Y2 + 16 * 16384 <= OFF_PO, "workspace map");
__device__ __forceinline__ int po_base(int e, unsigned near) { return OFF_PO + (e & 63) * 16384 + ((near >> (8 + (e >> 4))) & 1) * 8192; }
__device__ __forceinline__ int y1_line(int cg, int e, unsigned near) { return OFF_Y1 + (((cg >> 3) * 4 + (cg & 3)) * 2 + (e >> 6)) * 16384 + ((near >> (cg & 7)) & 1) * 8192 + (e & 63) * 128; }
__device__ __forceinline__ int y2_line(int cg, int f, unsigned near) { return OFF_Y2 + ((cg >> 4) * 4 + (cg & 3)) * 16384 + ((near >> (cg & 7)) & 1) * 8192 + ((cg >> 3) & 1) * 4096 + f * 128; }
constexpr int OFF_DIAG = 8 << 20;
constexpr int AUX_NT = 2, AUX_SC1 = 16;
constexpr unsigned SPIN_CAP = 1u << 20;
constexpr int OOB = 0x7ffffff0;

#define TD 4
#define WIN 5
#ifndef NVL
#define NVL 6
#endif
#define BAR_AT 0
#define MPOLL_AT 13
#define QPOLL_AT 24
#define ML_AT 10
#define QDIV 6
#define CODEPF 2
#define FPF 0
#define DEARLY 1
#define FWAIT 7u
#define TEND 18300
#define P0H 2200
#define P0L 0
#define TAILN 8u
#define FIRST 2
#define GU0 3500
#define GUTAU 500
#define STAG 1
#define NGE 14
#define NGT 7
#define TMOD 6200
#define TQ 9500
#define CATCH 300
#define BURST 2
#define ROT 1
#define ANCHOR 0
#define TLBW 0
#define HOLD1 0
#define HOLD2 0
#define HOLD3 0
#define HOLD4 0
#define PRET 0
#define PROBE 0
#define NPROBE 4
#define FLIP 0
#define TLANDH 3000
#define TLANDL 2400
#define GALL 7
#define WOE 0
#define TDELAY 0
#define THIN 0
#define FSYNC 1
#define TBASE 1
#define VNEAR 4
#define KARG 0
#define MIX 0
#define KB 2
#define VB 2
#define WTAU 0
#define WFIRST 4
#define FIRSTH FIRST
#define EBAR 0
#define SPF 0
#define LPRIO 0
#define MODREF 0
#define STLB 0
#define TT0 2300
#define TREF 1
#define FB 0
#define FB0 800
#define FBB 80
#define FBS 8
#define T0MISS 1200
#define LAND 0
#define LSLEEP 1
#define PSLEEP 8

constexpr int L_FLAG = 0;
constexpr int L_Q = 64;
constexpr int L_VF = 1152;
constexpr int L_DUE = 1184;
constexpr int L_ML = 1280;
constexpr int L_FW = 1408;
constexpr int L_ST = 2048;
constexpr int L_S1 = 1664;
constexpr int L_RED = 1792;
constexpr int L_H = 2048;
constexpr int L_ACT = 4096;
constexpr int L_DIAG = 5120;
constexpr int L_P = 8192;
constexpr int L_GU = L_P;
constexpr int L_V = 16384;
constexpr int L_W = 49152;
constexpr int LDS_BYTES = L_W + 112 * 1024;
static_assert(NGE == 14 && NVL <= 10 && LDS_BYTES <= 160 * 1024, "LDS map");

__device__ __forceinline__ float bflo(unsigned v) { return __uint_as_float(v << 16); }
__device__ __forceinline__ float bfhi(unsigned v) { return __uint_as_float(v & 0xffff0000u); }
__device__ __forceinline__ unsigned pk2(float lo, float hi) { bf16x2 v = {(__bf16)lo, (__bf16)hi}; return __builtin_bit_cast(unsigned, v); }
__device__ __forceinline__ float rbf(float f) { return (float)(__bf16)f; }
__device__ __forceinline__ float dot2(unsigned a, unsigned b, float acc) {
  return __builtin_amdgcn_fdot2_f32_bf16(__builtin_bit_cast(bf16x2, a), __builtin_bit_cast(bf16x2, b), acc, false);
}
__device__ __forceinline__ unsigned plo(unsigned a, unsigned b) { return __builtin_amdgcn_perm(b, a, 0x05040100u); }
__device__ __forceinline__ unsigned phi(unsigned a, unsigned b) { return __builtin_amdgcn_perm(b, a, 0x07060302u); }
__device__ __forceinline__ float silu(float x) { return x * __builtin_amdgcn_rcpf(1.f + __builtin_amdgcn_exp2f(x * -1.4426950408889634f)); }
template <int CTRL> __device__ __forceinline__ float dpp(float x) {
  return __builtin_bit_cast(float, __builtin_amdgcn_mov_dpp(__builtin_bit_cast(int, x), CTRL, 0xf, 0xf, true));
}
constexpr int XOR1 = 0xB1, XOR2 = 0x4E, XOR7 = 0x141, ROR8 = 0x128, ROR4 = 0x124;
__device__ __forceinline__ float tr32(float a, float b) {
  auto s = __builtin_amdgcn_permlane32_swap(__float_as_uint(a), __float_as_uint(b), false, false);
  return __uint_as_float(s[0]) + __uint_as_float(s[1]);
}
__device__ __forceinline__ float tr16(float a, float b) {
  auto s = __builtin_amdgcn_permlane16_swap(__float_as_uint(a), __float_as_uint(b), false, false);
  return __uint_as_float(s[0]) + __uint_as_float(s[1]);
}
template <int CTRL> __device__ __forceinline__ float trd(float a, float b, bool bit) {
  const float keep = bit ? b : a, send = bit ? a : b;
  return keep + dpp<CTRL>(send);
}
__device__ __forceinline__ float max32(float x) {
  auto s = __builtin_amdgcn_permlane32_swap(__float_as_uint(x), __float_as_uint(x), false, false);
  return fmaxf(__uint_as_float(s[0]), __uint_as_float(s[1]));
}
__device__ __forceinline__ float max16(float x) {
  auto s = __builtin_amdgcn_permlane16_swap(__float_as_uint(x), __float_as_uint(x), false, false);
  return fmaxf(__uint_as_float(s[0]), __uint_as_float(s[1]));
}
__device__ __forceinline__ float wave_sum(float x) {
  x += dpp<XOR1>(x); x += dpp<XOR2>(x); x += dpp<ROR4>(x); x += dpp<ROR8>(x); x = tr16(x, x); return tr32(x, x);
}
__device__ __forceinline__ rsrc_t mkrsrc(const void* p, int bytes) { return __builtin_amdgcn_make_buffer_rsrc((void*)p, 0, bytes, 0x00020000); }
__device__ __forceinline__ v4u ldnt(rsrc_t r, int voff, int soff) { return __builtin_amdgcn_raw_buffer_load_b128(r, voff, soff, AUX_NT); }
__device__ __forceinline__ v4u ldsc(rsrc_t r, int voff) { return __builtin_amdgcn_raw_buffer_load_b128(r, voff, 0, AUX_SC1); }
__device__ __forceinline__ v2u ldsc2(rsrc_t r, int voff) { return __builtin_amdgcn_raw_buffer_load_b64(r, voff, 0, AUX_SC1); }
__device__ __forceinline__ void stsc(rsrc_t r, int voff, v4u v) { __builtin_amdgcn_raw_buffer_store_b128(v, r, voff, 0, AUX_SC1); }
__device__ __forceinline__ void stsc2(rsrc_t r, int voff, v2u v) { __builtin_amdgcn_raw_buffer_store_b64(v, r, voff, 0, AUX_SC1); }
template <int N> __device__ __forceinline__ void waitvm() { asm volatile("s_waitcnt vmcnt(%0)" :: "i"(N) : "memory"); }
#define SB() __builtin_amdgcn_sched_barrier(0)
#define PIN(x) asm volatile("" : "+v"(x))
__device__ __forceinline__ void ldswait() { asm volatile("s_waitcnt lgkmcnt(0)" ::: "memory"); }
__device__ __forceinline__ void wgbar() { SB(); asm volatile("s_waitcnt lgkmcnt(0)" ::: "memory"); __builtin_amdgcn_s_barrier(); asm volatile("" ::: "memory"); SB(); }

template <int N, int SLP = 0> __device__ __forceinline__ void gather(rsrc_t ws, int (&off)[N], v2u (&val)[N], unsigned tag) {
#pragma unroll
  for (int k = 0; k < N; ++k) val[k] = v2u{0u, 0u};
#pragma unroll 1
  for (unsigned spins = 0; spins < SPIN_CAP; ++spins) {
    v4u g[N];
#pragma unroll
    for (int k = 0; k < N; ++k) g[k] = ldsc(ws, off[k]);
    bool open = false;
#pragma unroll
    for (int k = 0; k < N; ++k) {
      const bool m = (g[k].y == tag) & (g[k].w == tag);
      val[k].x = m ? g[k].x : val[k].x; val[k].y = m ? g[k].z : val[k].y; off[k] = m ? OOB : off[k];
      open |= off[k] != OOB;
    }
    if (!__any(open)) break;
    if (SLP) __builtin_amdgcn_s_sleep(SLP);
    asm volatile("" ::: "memory");
  }
}
template <int N> __device__ __forceinline__ void gather_all(rsrc_t ws, const int (&off)[N], v2u (&val)[N], unsigned tag, u64 skip = 0) {
  unsigned spins = SPIN_CAP;
  u64 ok;
#pragma unroll 1
  do {
    asm volatile("" ::: "memory");
    v4u g[N];
#pragma unroll
    for (int k = 0; k < N; ++k) g[k] = ldsc(ws, off[k]);
    ok = --spins ? 0ull : ~0ull;
    u64 a = ~0ull;
#pragma unroll
    for (int k = 0; k < N; ++k) {
      const u64 m = __builtin_amdgcn_ballot_w64(g[k].y == tag) & __builtin_amdgcn_ballot_w64(g[k].w == tag);
      a &= k == N - 1 ? m | skip : m;
      val[k] = v2u{g[k].x, g[k].z};
    }
    ok |= a;
  } while (ok != ~0ull);
}
__device__ __forceinline__ unsigned gather8(rsrc_t ws, int off, unsigned tag) {
  unsigned spins = SPIN_CAP;
  u64 ok; v2u g;
#pragma unroll 1
  do {
    asm volatile("" ::: "memory");
    g = ldsc2(ws, off);
    ok = --spins ? 0ull : ~0ull;
    ok |= __builtin_amdgcn_ballot_w64(g.y == tag);
  } while (ok != ~0ull);
  return g.x;
}
#define PINS(x) asm volatile("" : "+s"(x))
__device__ __forceinline__ void lds_wait_ge(volatile LAS unsigned* word, unsigned want) {
#pragma unroll 1
  for (unsigned spins = 0; spins < (SPIN_CAP << 4); ++spins) { if (*word >= want) break; __builtin_amdgcn_s_sleep(LSLEEP); }
  asm volatile("" ::: "memory");
  SB();
}
typedef short v8s __attribute__((ext_vector_type(8)));
typedef __bf16 v8bf __attribute__((ext_vector_type(8)));
__device__ __forceinline__ v4f mfma(v4u a, v4u b, v4f c) {
  return __builtin_amdgcn_mfma_f32_16x16x32_bf16(__builtin_bit_cast(v8bf, a), __builtin_bit_cast(v8bf, b), c, 0, 0, 0);
}
__device__ __forceinline__ v4u sel4(bool on, v4u v) { return v4u{on ? v.x : 0u, on ? v.y : 0u, on ? v.z : 0u, on ? v.w : 0u}; }
__device__ __forceinline__ void tile_cols(const v4u* src, v4u (&wc)[8]) {
#pragma unroll
  for (int k = 0; k < 4; ++k) {
    wc[2 * k] = v4u{plo(src[0][k], src[1][k]), plo(src[2][k], src[3][k]), plo(src[4][k], src[5][k]), plo(src[6][k], src[7][k])};
    wc[2 * k + 1] = v4u{phi(src[0][k], src[1][k]), phi(src[2][k], src[3][k]), phi(src[4][k], src[5][k]), phi(src[6][k], src[7][k])};
    PIN(wc[2 * k]); PIN(wc[2 * k + 1]);
  }
}
template <bool TWO> __device__ __forceinline__ v4f tile_gemv(const v4u (&w0)[8], v4u in0, const v4u (&w1)[8], v4u in1) {
  v4f d[8], r;
#pragma unroll
  for (int c = 0; c < 10; ++c) {
    if (c < 8) { d[c] = mfma(w0[c], in0, v4f{0.f, 0.f, 0.f, 0.f}); if (TWO) d[c] = mfma(w1[c], in1, d[c]); }
    if (c == 2) r = d[0];
    if (c > 2) {
      const bool m = __builtin_amdgcn_inverse_ballot_w64(0x0101010101010101ull << (c - 2));
      r = v4f{m ? d[c - 2].x : r.x, m ? d[c - 2].y : r.y, m ? d[c - 2].z : r.z, m ? d[c - 2].w : r.w};
      PIN(r);
    }
    SB();
  }
  return r;
}
__device__ __forceinline__ void publish_tile(rsrc_t ws, v4f y, int so, int so2, unsigned tag) {
  const bool hi = __builtin_amdgcn_inverse_ballot_w64(0xff00ff00ff00ff00ull);
  const float r0 = dpp<ROR8>(hi ? y.x : y.y), r1 = dpp<ROR8>(hi ? y.z : y.w);
  __builtin_amdgcn_raw_buffer_store_b128(v4u{__float_as_uint(hi ? r0 : y.x), tag, __float_as_uint(hi ? y.y : r0), tag}, ws, so, 0, AUX_SC1);
  __builtin_amdgcn_raw_buffer_store_b128(v4u{__float_as_uint(hi ? r1 : y.z), tag, __float_as_uint(hi ? y.w : r1), tag}, ws, so2, 0, AUX_SC1);
}

struct Params {
  const u16 *x, *K, *V, *wq, *wo, *wg, *wu, *wd, *c, *wmod;
  u16* out;
  unsigned char* ws;
};

#define STAMP(i) do { } while (0)
#define DIAG_INIT() do { } while (0)
#define DIAG_FLUSH() do { } while (0)

constexpr int issued_before(int n) {
  int a = (32 + NVL + VB + (n < 32 ? n : 31) / QDIV) / VB * VB, b = (n + WIN) / VB * VB;
  int m = a > b ? a : b;
  return m > 64 ? 64 : m;
}
__device__ __forceinline__ void lds_spin_ge(volatile LAS unsigned* word, unsigned want) {
#pragma unroll 1
  for (unsigned spins = 0; spins < (SPIN_CAP << 5); ++spins) { if (*word >= want) break; }
  asm volatile("" ::: "memory");
  SB();
}
__device__ __forceinline__ void pace(unsigned& due, unsigned& lim, unsigned step, unsigned fast, unsigned burst, unsigned t0) {
  unsigned tmp;
  asm volatile("s_memrealtime vcc\n\ts_add_u32 %0, %0, %3\n\ts_add_u32 %1, %1, %4\n\ts_max_i32 %1, %1, %0\n\ts_waitcnt lgkmcnt(0)\n\t"
               "s_sub_u32 vcc_lo, vcc_lo, %6\n\ts_lshl_b32 vcc_lo, vcc_lo, 4\n\ts_sub_u32 %2, vcc_lo, %5\n\ts_max_i32 %1, %1, %2\n\t"
               ".Lpw%=:\n\ts_cmp_ge_i32 vcc_lo, %1\n\ts_cbranch_scc1 .Lpd%=\n\ts_memrealtime vcc\n\ts_waitcnt lgkmcnt(0)\n\t"
               "s_sub_u32 vcc_lo, vcc_lo, %6\n\ts_lshl_b32 vcc_lo, vcc_lo, 4\n\ts_branch .Lpw%=\n.Lpd%=:"
               : "+s"(due), "+s"(lim), "=&s"(tmp) : "s"(step), "s"(fast), "s"(burst), "s"(t0), "i"(LPRIO * 16 / 10), "i"(LPRIO * 32 / 10) : "vcc", "scc", "memory");
}
__device__ __forceinline__ void wait_until(unsigned due) {
  while ((int)((unsigned)__builtin_amdgcn_s_memrealtime() - due) < 0) __builtin_amdgcn_s_sleep(1);
  SB();
}
__device__ __forceinline__ void lds_inc(LAS unsigned char* L, int word, int lane) {
  ldswait();
  if (lane == 0) __hip_atomic_fetch_add((LAS unsigned*)(L + L_FLAG + word * 4), 1u, __ATOMIC_RELAXED, __HIP_MEMORY_SCOPE_WORKGROUP);
  SB();
}
constexpr v4f Z4 = {0.f, 0.f, 0.f, 0.f};

extern "C" __global__ void __launch_bounds__(NT, 3) mk_kernel(Params p) {
  extern __shared__ __attribute__((aligned(16))) unsigned char smem[];
  LAS unsigned char* L = (LAS unsigned char*)smem;
  const int bid = blockIdx.x, t = threadIdx.x, lane = t & 63, w = __builtin_amdgcn_readfirstlane(t >> 6);
  const int x = bid & 7, j = bid >> 3;
  const int hkv = x, b = j >> 4, s = j & 15;
  const int lo16 = lane * 16, g4 = lane >> 4, n16 = lane & 15, hi3 = lane >> 3, lo3 = lane & 7;
  volatile LAS unsigned* flag = (volatile LAS unsigned*)(L + L_FLAG);
  const bool roleF = (j >= 13 && j < 16) || (j == 12 && x >= 4);
  const int f0 = 3 * x + max(0, x - 4), f = f0 + (j >= 13 ? j - 13 : 3);
  unsigned tent; { u64 te_; asm volatile("s_memrealtime %0" : "=s"(te_)); asm volatile("s_waitcnt lgkmcnt(0)" : "+s"(te_) : "s"(p.K), "s"(p.ws)); tent = (unsigned)te_; }
  int probeX = 0;
#define NOWT() ((int)((unsigned)__builtin_amdgcn_s_memrealtime() - tent))
  asm volatile("" :: "s"(p.K), "s"(p.ws) : "memory");
  SB();
  const unsigned tk0 = (unsigned)__builtin_amdgcn_s_memrealtime();
  DIAG_INIT();
  STAMP(0);

  if (w < 4) {
    __builtin_amdgcn_s_setprio(2);
    const unsigned th0 = (unsigned)__builtin_amdgcn_s_memrealtime();
    const rsrc_t ws = mkrsrc(p.ws, WS_BYTES);
    const bool roleB = j < 8;
    const bool roleA = (j >= 8 && j < 12) || (j == 12 && x < 3);
    const bool roleS = j >= 16;
    const int e = x * 16 + s;
    const bool on = t < D / 2;
    LAS unsigned* H = (LAS unsigned*)(L + L_H + t * 4);
    const int a0 = 4 * x + min(x, 3), unit = roleB ? x * 8 + j : a0 + j - 8;
    const int vd = (w * 16 + n16 < NCG) ? g4 * 8 * D * 2 + w * 256 + n16 * 16 : OOB;
    const int pitch = roleB ? HQ * DH * 2 : roleA ? 6 * D * 2 : D * 2;
    const rsrc_t rw = mkrsrc(roleB ? p.wq : roleA ? p.wmod : roleF ? p.wd + (size_t)f * 64 * D : p.wo + (size_t)e * 32 * D, ((roleA | roleB) ? D : roleF ? 64 : 32) * pitch);
    const int va = (128 * w + 8 * hi3) * pitch + unit * 128 + lo3 * 16;
    const int vo = va, vo2 = w < 3 ? va + 64 * pitch : OOB;
    const u16* gu = ((lane & 8) ? p.wu : p.wg) + (size_t)(8 * g4) * FF + (roleF ? f : 0) * 64 + lo3 * 8;
#define GU_PIECE(i_) { const int q = (i_) < 56 ? ((i_) >> 3) * 16 + ((i_) & 7) : (((i_) - 56) >> 3) * 16 + 8 + ((i_) & 7); const u16* src_ = gu + (size_t)(32 * ((q) >> 3) + ((q) & 7)) * FF; unsigned keep_; \
      asm volatile("s_mov_b32 %0, m0\n\ts_mov_b32 m0, %1\n\ts_nop 0\n\tglobal_load_lds_dwordx4 %2, off nt\n\ts_mov_b32 m0, %0" \
                   : "=&s"(keep_) : "s"((unsigned)(size_t)(L + L_W) + (i_) * 1024), "v"(src_) : "memory"); }
#define WPACE(r) if (WTAU) { const unsigned d_ = tk0 + (KARG + (4 * (r) + w) * WTAU / 4) / 10; while ((int)((unsigned)__builtin_amdgcn_s_memrealtime() - d_) < 0) { } }
    if (t < 16) flag[t] = 0u;
    if (t < 2) *(LAS unsigned*)(L + L_VF + t * 4) = 0u;
    unsigned spf = 0;
    if (SPF) {
      const int idx = (j * 4 + w) * 56 + min(lane, 55), na = x < 3 ? 5 : 4, nf = x < 4 ? 3 : 4;
      const int i2 = idx - 3584, i3 = min(i2 - 448 * na, 448 * nf - 1);
      const int ra = x < 3 ? i2 / 5 : i2 >> 2, rf = x < 4 ? i3 / 3 : i3 >> 2;
      const u16* a = idx < 3584 ? p.wq + (size_t)(idx >> 3) * (HQ * DH) + (x * 8 + (idx & 7)) * 64
                   : i3 < 0 ? p.wmod + (size_t)ra * (6 * D) + (a0 + i2 - ra * na) * 64
                   : (rf < 224 ? p.wg : p.wu) + (size_t)(64 * ((rf % 224) >> 5) + (rf % 224 & 31)) * FF + (f0 + i3 - rf * nf) * 64;
      spf = __builtin_nontemporal_load((const GAS unsigned*)a);
      SB();
    }
    if (EBAR) wgbar();
    v4u wt[16], xr0 = {0u, 0u, 0u, 0u}, xr1 = xr0;
    unsigned xo0 = 0u, xo1 = 0u;
    if (roleA | roleB) {
      const rsrc_t rx = mkrsrc(roleB ? p.x : p.c, 2 * D * 2);
      const int xo = on ? t * 4 : OOB, xv = (roleB && lane < 56) ? lo16 : OOB;
      xo0 = __builtin_amdgcn_raw_buffer_load_b32(rx, xo, 0, 0); xo1 = __builtin_amdgcn_raw_buffer_load_b32(rx, xo, D * 2, 0);
      xr0 = __builtin_amdgcn_raw_buffer_load_b128(rx, xv, 0, 0); xr1 = __builtin_amdgcn_raw_buffer_load_b128(rx, xv, D * 2, 0);
#pragma unroll
      for (int r = 0; r < 16; ++r) { if (SPF && r == 0) wait_until(tk0 + SPF / 10); if (r >= WFIRST) WPACE(r) wt[r] = ldnt(rw, r < 8 ? vo : vo2, (r & 7) * pitch); SB(); }
    } else if (roleF) {
#pragma unroll 1
      for (int n = 0; n < NGE; ++n) { if (SPF && n == 0) wait_until(tk0 + SPF / 10); if (n >= WFIRST) WPACE(n) GU_PIECE(4 * n + w) }
    }
    if (!EBAR) wgbar();
    unsigned ep;
    asm volatile("s_load_dword %0, %1, 0x0\n\ts_waitcnt lgkmcnt(0)" : "=s"(ep) : "s"(p.ws) : "memory");
    const unsigned tag = ep + 1u ? ep + 1u : 1u;
    unsigned tcom = tk0;
    if (w >= 2 || roleB) {
      wait_until(tk0 + TT0 / 10);
      u64 v[8];
      asm volatile("s_load_dwordx2 %0, %8, %9\n\ts_load_dwordx2 %1, %8, %9+128\n\ts_load_dwordx2 %2, %8, %9+256\n\ts_load_dwordx2 %3, %8, %9+384\n\t"
                   "s_load_dwordx2 %4, %8, %9+512\n\ts_load_dwordx2 %5, %8, %9+640\n\ts_load_dwordx2 %6, %8, %9+768\n\ts_load_dwordx2 %7, %8, %9+896\n\ts_waitcnt lgkmcnt(0)"
                   : "=&s"(v[0]), "=&s"(v[1]), "=&s"(v[2]), "=&s"(v[3]), "=&s"(v[4]), "=&s"(v[5]), "=&s"(v[6]), "=&s"(v[7]) : "s"(p.ws), "i"(OFF_T0) : "memory");
      int mx = 0;
#pragma unroll
      for (int k = 0; k < 8; ++k) mx = max(mx, (unsigned)(v[k] >> 32) == tag ? (int)((unsigned)v[k] - tk0) : T0MISS / 10);
      tcom = tk0 + min(mx, 300);
      if (t == 128) *(LAS int*)(L + L_VF) = (int)(tcom - tk0) << 4;
      STAMP(31);
    }
    auto control = [&]() {
      asm volatile("" ::: "memory");
      const v2u sv = ldsc2(ws, lane == 0 ? OFF_SH + (bid & 31) * 128 : OOB);
      if ((unsigned)__builtin_amdgcn_readfirstlane(sv.y) == tag) {
        const int known = (int)min((unsigned)__builtin_amdgcn_readfirstlane(sv.x), 6400u);
        if (lane == 0) *(LAS int*)(L + L_VF) = ((int)(tcom - tk0) << 4) + known;
        int dm = *(volatile LAS int*)(L + L_DUE + lo3 * 4);
        dm = min(dm, __builtin_amdgcn_mov_dpp(dm, XOR1, 0xf, 0xf, true)); dm = min(dm, __builtin_amdgcn_mov_dpp(dm, XOR2, 0xf, 0xf, true)); dm = min(dm, __builtin_amdgcn_mov_dpp(dm, XOR7, 0xf, 0xf, true));
        dm = __builtin_amdgcn_readfirstlane(dm);
        const int now = (int)((unsigned)__builtin_amdgcn_s_memrealtime() - tcom) << 4;
        const int need = now - dm - FB0 * 16 / 10 - (TEND * 16 / 10 - dm) * FBB / 1000;
        if (need > known + 160) {
          const unsigned req = known + (need - known) * FB / 10;
          if (lane < 32) __hip_atomic_fetch_max((GAS unsigned*)(p.ws + OFF_SH + lane * 128), req, __ATOMIC_RELAXED, __HIP_MEMORY_SCOPE_AGENT);
        }
      }
      __builtin_amdgcn_s_sleep(FBS);
    };
    v4u wc0[8], wc1[8];
#define UNIT_GEMV(UNIT, ISA, CNT) { \
      ldswait(); \
      const LAS v4u* HA = (const LAS v4u*)(L + L_H + (lane & 1) * 1024 + (128 * w + 16 * g4 + (lane & 4) * 2) * 2); \
      const v4u ha0 = HA[0], ha1 = HA[8]; \
      v4f y[8]; \
_Pragma("unroll") \
      for (int c = 0; c < 8; ++c) y[c] = mfma(ha0, wc0[c], Z4); \
_Pragma("unroll") \
      for (int c = 0; c < 8; ++c) y[c] = mfma(ha1, wc1[c], y[c]); \
      if (lane < 8 || (lane >= 24 && lane < 32)) { \
        LAS v2f* G = (LAS v2f*)(L + L_W + w * 1024 + (lane & 8) * 64 + lo3 * 64); \
_Pragma("unroll") \
        for (int c = 0; c < 8; ++c) G[c] = v2f{y[c].x, y[c].y}; \
      } \
      lds_inc(L, 2, lane); \
      if (w == 0) { \
        lds_wait_ge(flag + 2, CNT); \
        const LAS v4f* G = (const LAS v4f*)(L + L_W + (lane >> 5) * 2048 + (lane & 31) * 16); \
        const v4f a = (G[0] + G[32]) + (G[64] + G[96]); \
        const float e0 = (ISA) ? tr32(a.x, a.z) : tr32(a.x, a.y), e1 = (ISA) ? tr32(a.y, a.w) : tr32(a.z, a.w); \
        const int so = (ISA) ? OFF_MOD + (64 * (UNIT) + 2 * (lane & 31) + (lane >> 5)) * 8 : OFF_Q + ((lane >> 5) * 2048 + (UNIT) * 32 + (lane & 31)) * 8; \
        stsc2(ws, so, v2u{pk2(e0, e1), tag}); \
        SB(); \
        STAMP(1); \
      } \
    }
    float n00 = 0.f, n01 = 0.f, n10 = 0.f, n11 = 0.f;
    if (roleA) {
      H[0] = pk2(silu(bflo(xo0)), silu(bfhi(xo0))); H[256] = pk2(silu(bflo(xo1)), silu(bfhi(xo1)));
    } else if (roleB) {
      float s0 = 0.f, s1 = 0.f;
#pragma unroll
      for (int k = 0; k < 4; ++k) { s0 += bflo(xr0[k]) + bfhi(xr0[k]); s1 += bflo(xr1[k]) + bfhi(xr1[k]); }
      const float m0 = wave_sum(s0) * (1.f / D), m1 = wave_sum(s1) * (1.f / D);
      float v0 = 0.f, v1 = 0.f;
#pragma unroll
      for (int k = 0; k < 4; ++k) {
        const float a = bflo(xr0[k]) - m0, c = bfhi(xr0[k]) - m0, d = bflo(xr1[k]) - m1, q = bfhi(xr1[k]) - m1;
        v0 += a * a + c * c; v1 += d * d + q * q;
      }
      if (lane >= 56) { v0 = 0.f; v1 = 0.f; }
      v0 = wave_sum(v0) * (1.f / D); v1 = wave_sum(v1) * (1.f / D);
      const float r0 = __builtin_amdgcn_rcpf(rbf(__builtin_amdgcn_sqrtf(rbf(rbf(v0) + 1e-5f)))), r1 = __builtin_amdgcn_rcpf(rbf(__builtin_amdgcn_sqrtf(rbf(rbf(v1) + 1e-5f))));
      const float mb0 = rbf(m0), mb1 = rbf(m1);
      n00 = rbf(rbf(bflo(xo0) - mb0) * r0); n01 = rbf(rbf(bfhi(xo0) - mb0) * r0); n10 = rbf(rbf(bflo(xo1) - mb1) * r1); n11 = rbf(rbf(bfhi(xo1) - mb1) * r1);
    }
    PIN(n00); PIN(n01); PIN(n10); PIN(n11);
    if (roleA | roleB) { tile_cols(wt, wc0); tile_cols(wt + 8, wc1); }
    asm volatile("" :: "v"(spf));
    __builtin_amdgcn_s_waitcnt(0x0F70);
    waitvm<0>();
    STAMP(19);
    if (PX == 5) probeX = 5 * NOWT();
    const unsigned tland = (unsigned)__builtin_amdgcn_s_memrealtime() - ((roleA | roleB | roleF) ? TLANDH : TLANDL) / 10;
    const unsigned tref = (int)(tland - th0) > 0 ? tland : th0;
    if (roleB) {
      if (FB && w == 2) while ((int)((unsigned)__builtin_amdgcn_s_memrealtime() - (tcom + (TMOD - 1200) / 10)) < 0) control();
      wait_until(tcom + TMOD / 10);
      STAMP(26);
      int off[2]; v2u g[2];
      off[0] = on ? OFF_MOD + t * 16 : OOB; off[1] = on ? OFF_MOD + D * 8 + t * 16 : OOB;
      gather<2, PSLEEP>(ws, off, g, tag);
      STAMP(2);
      if (PX == 6) probeX = 3 * NOWT();
      const unsigned c0 = pk2(1.f + bflo(g[1].x), 1.f + bflo(g[1].y)), c1 = pk2(1.f + bfhi(g[1].x), 1.f + bfhi(g[1].y));
      const unsigned u0 = pk2(n00 * bflo(c0), n01 * bfhi(c0)), u1 = pk2(n10 * bflo(c1), n11 * bfhi(c1));
      H[0] = on ? pk2(bflo(u0) + bflo(g[0].x), bfhi(u0) + bflo(g[0].y)) : 0u; H[256] = on ? pk2(bflo(u1) + bfhi(g[0].x), bfhi(u1) + bfhi(g[0].y)) : 0u;
    }
    if (roleA | roleB) UNIT_GEMV(unit, roleA, 4u)
    if (w == 1) {
      u64 pc; unsigned back, l1, l2, l3;
      asm volatile("s_getpc_b64 %0\n.Lmk_anchor:\n\ts_mov_b32 %1, .Lmk_anchor-mk_kernel\n\ts_mov_b32 %2, .Lmk_end1-mk_kernel\n\ts_mov_b32 %3, .Lmk_end2-mk_kernel\n\ts_mov_b32 %4, .Lmk_end4-mk_kernel"
                   : "=s"(pc), "=s"(back), "=s"(l1), "=s"(l2), "=s"(l3));
      const unsigned len = max(l1, max(l2, l3));
      const unsigned lines = (len + 127u) >> 7, per = (lines + 31u) >> 5, li = min(j * per + min((unsigned)lane, per - 1u), lines - 1u), co = li * 128u;
      const unsigned cpf = *(const GAS unsigned*)(pc - back + co);
      asm volatile("" :: "v"(cpf));
      if (CODEPF > 1) {
        lds_wait_ge(flag + 6, 1u);
        const unsigned cp2 = __hip_atomic_load((const GAS unsigned*)(pc - back + co), __ATOMIC_RELAXED, __HIP_MEMORY_SCOPE_AGENT);
        asm volatile("" :: "v"(cp2));
      }
    }
    if (w == 0 && j == 31 && x < 4) {
      __builtin_amdgcn_raw_buffer_store_b32(0u, ws, n16 < NPROBE ? OFF_PRB + (g4 & 1) * 8192 + (g4 >> 1) * (OFF_PRB1 - OFF_PRB) + x * 2048 + n16 * 128 : OOB, 0, AUX_SC1);
      waitvm<0>();
      unsigned mn[4] = {~0u, ~0u, ~0u, ~0u};
#pragma unroll 1
      for (int r = 0; r < NPROBE; ++r) {
        const int pb = __builtin_amdgcn_readfirstlane(OFF_PRB + x * 2048 + r * 128);
#pragma unroll
        for (int k = 0; k < 4; ++k) {
          u64 a, c; unsigned v;
          asm volatile("s_memtime %0\n\ts_waitcnt lgkmcnt(0)\n\ts_load_dword %2, %3, %4\n\ts_waitcnt lgkmcnt(0)\n\ts_memtime %1\n\ts_waitcnt lgkmcnt(0)"
                       : "=&s"(a), "=&s"(c), "=&s"(v) : "s"(p.ws), "s"(pb + (k & 1) * 8192 + (k >> 1) * (OFF_PRB1 - OFF_PRB)) : "memory");
          mn[k] = min(mn[k], (unsigned)(c - a));
        }
      }
      const unsigned bits = ((mn[1] < mn[0]) | (mn[3] < mn[2]) << 1) ^ FLIP;
      stsc(ws, lane == 0 ? OFF_PREF + x * 128 : OOB, v4u{bits, tag, bits, tag});
    }
    if (FB && w == 2) {
#pragma unroll 1
      for (int it = 0; it < 4096 && flag[6] < 8u; ++it) control();
    }
    if (w == 3) {
      wait_until(tcom + TQ / 10);
      STAMP(25);
      {
        int off[2]; v2u g[2];
        off[0] = OFF_Q + (b * 2048 + hkv * 256) * 8 + lo16; off[1] = off[0] + 1024;
        gather<2, PSLEEP>(ws, off, g, tag);
        *(LAS v2u*)(L + L_Q + lane * 8) = g[0]; *(LAS v2u*)(L + L_Q + 512 + lane * 8) = g[1];
        if (lane == 0) flag[1] = tag;
        lds_inc(L, 0, lane);
      }
      STAMP(4);
      lds_wait_ge(flag + 3, 8u);
      const int h = g4 & 1;
      const v2f ml = *(const LAS v2f*)(L + L_ML + (h * 8 + lo3) * 8);
      float M = ml.x;
      M = fmaxf(M, dpp<XOR1>(M)); M = fmaxf(M, dpp<XOR2>(M)); M = fmaxf(M, dpp<XOR7>(M));
      float Ls = __builtin_amdgcn_exp2f(ml.x - M) * ml.y;
      Ls += dpp<XOR1>(Ls); Ls += dpp<XOR2>(Ls); Ls += dpp<XOR7>(Ls);
      const int mo = OFF_ML + (b * 8 + hkv) * 2048 + h * 16;
      stsc(ws, (lane == 0 || lane == 16) ? mo + s * 128 : OOB, v4u{__float_as_uint(M), tag, __float_as_uint(Ls), tag});
      waitvm<0>();
      lds_wait_ge(flag + 6, 1u);
      int off[2]; v2u g[2];
      off[0] = mo + n16 * 128; off[1] = lane < 16 ? OFF_PREF + (lane & 3) * 128 : OOB;
      gather<2, PSLEEP>(ws, off, g, tag);
      const unsigned nb = (unsigned)__ballot(((g[1].x >> (lane >> 3)) ^ (lane >> 2)) & 1) & 0xffffu;
      if (lane == 0) flag[8] = nb;
      float G = __uint_as_float(g[0].x);
      G = fmaxf(G, dpp<XOR1>(G)); G = fmaxf(G, dpp<XOR2>(G)); G = fmaxf(G, dpp<ROR4>(G)); G = fmaxf(G, dpp<ROR8>(G));
      float T = __builtin_amdgcn_exp2f(__uint_as_float(g[0].x) - G) * __uint_as_float(g[0].y);
      T += dpp<XOR1>(T); T += dpp<XOR2>(T); T += dpp<ROR4>(T); T += dpp<ROR8>(T);
      if (lane < 32 && !(lane & 8)) *(LAS float*)(L + L_FW + (h * 8 + lo3) * 4) = __builtin_amdgcn_exp2f(ml.x - G) / T;
      lds_inc(L, 5, lane);
      STAMP(3);
    }
    unsigned gav = 0;
    const int cg = j * 8 + x;
    unsigned xin0 = 0, xin1 = 0;
    if (j < 7 && w == 0) {
      lds_wait_ge(flag + 6, 1u);
      xin0 = p.x[8 * cg + lo3]; xin1 = p.x[D + 8 * cg + lo3];
      gav = gather8(ws, OFF_MOD + (2 * D + 8 * cg + lo3) * 8, tag);
      PIN(xin0); PIN(xin1);
    }
    lds_wait_ge(flag + 5, 1u);
    const unsigned near = __builtin_amdgcn_readfirstlane(flag[8]);
    if (roleS | roleF) {
      waitvm<0>();
      lds_wait_ge(flag + 4, TAILN);
      if (TDELAY) __builtin_amdgcn_s_sleep(TDELAY);
      if (roleS) {
#pragma unroll
        for (int r = 0; r < 8; ++r) wt[r] = ldnt(rw, vd, r * pitch);
        SB();
      } else {
#pragma unroll 1
        for (int n = NGE; n < NGE + NGT; ++n) GU_PIECE(4 * n + w)
      }
    }
    {
      const int h = w >> 1, dp = t & 127;
      v4f f0 = *(const LAS v4f*)(L + L_FW + h * 32), f1 = *(const LAS v4f*)(L + L_FW + h * 32 + 16);
      int po = po_base((2 * hkv + h) * 8 + (dp >> 4), near) + (b * 16 + s) * 256 + (dp & 15) * 16;
      PIN(f0); PIN(f1); PIN(po);
      wgbar();
      const LAS v2f* O = (const LAS v2f*)(L + L_V + h * 1024 + dp * 8);
      v2f v[8];
#pragma unroll
      for (int k = 0; k < 8; ++k) v[k] = O[k * 512];
      SB();
      const v2f o = ((v[0] * f0.x + v[1] * f0.y) + (v[2] * f0.z + v[3] * f0.w)) + ((v[4] * f1.x + v[5] * f1.y) + (v[6] * f1.z + v[7] * f1.w));
      stsc(ws, po, v4u{__float_as_uint(o.x), tag, __float_as_uint(o.y), tag});
      SB();
    }
    STAMP(9);
    if (PX == 3) probeX = 2 * NOWT();
    const int cg0 = 16 * w + 4 * g4;

    if (roleS) {
      tile_cols(wt, wc0);
      STAMP(20);
      int off[2]; v2u g[2];
      off[0] = po_base(e, near) + (w >> 1) * 4096 + hi3 * 256 + (w & 1) * 128 + lo3 * 16; off[1] = off[0] + 2048;
      int so = cg0 < NCG ? y1_line(cg0 + (hi3 & 1), e, near) + lo3 * 16 : OOB, so2 = cg0 < NCG ? y1_line(cg0 + 2 + (hi3 & 1), e, near) + lo3 * 16 : OOB;
      LAS u16* ow = (LAS u16*)(L + L_S1 + w * 32 + lo3 * 4 + (lane >> 5) * 2);
      const LAS v4u* orr = (const LAS v4u*)(L + L_S1 + (lane & 8) * 8 + g4 * 16);
      PIN(so); PIN(so2); PIN(ow); PIN(orr);
      gather_all<2>(ws, off, g, tag);
      STAMP(10);
      float v = tr32(__uint_as_float(g[0].x) + __uint_as_float(g[1].x), __uint_as_float(g[0].y) + __uint_as_float(g[1].y));
      v = tr16(v, v); v += dpp<ROR8>(v);
      *ow = (u16)pk2(v, v);
      wgbar();
      const v4u ob = *orr;
      publish_tile(ws, tile_gemv<false>(wc0, ob, wc0, ob), so, so2, tag);
      STAMP(11);
      asm volatile(".Lmk_end1:");
    } else if (roleF) {
#pragma unroll 1
      for (int n = NGE + NGT; n < 28; ++n) GU_PIECE(4 * n + w)
      if (FSYNC) { lds_inc(L, 7, lane); lds_spin_ge(flag + 7, 4u); }
#pragma unroll
      for (int r = 0; r < 16; ++r) wt[r] = ldnt(rw, vd, ((r >> 3) * 32 + (r & 7)) * pitch);
      SB();
      waitvm<16>();
      STAMP(14);
      wgbar();
      int so = cg0 < NCG ? y2_line(cg0 + (hi3 & 1), f, near) + lo3 * 16 : OOB, so2 = cg0 < NCG ? y2_line(cg0 + 2 + (hi3 & 1), f, near) + lo3 * 16 : OOB;
      PIN(so); PIN(so2);
      tile_cols(wt, wc0); tile_cols(wt + 8, wc1);
      STAMP(20);
      lds_wait_ge(flag + 2, 7u);
      v4f cg_ = Z4, cu_ = Z4;
#pragma unroll
      for (int k = 0; k < 7; ++k) { cg_ += *(const LAS v4f*)(L + L_V + k * 4096 + 1024 + lane * 32); cu_ += *(const LAS v4f*)(L + L_V + k * 4096 + 1024 + lane * 32 + 16); }
      PIN(cg_); PIN(cu_);
#pragma unroll 1
      for (unsigned spins = 0; spins < (SPIN_CAP << 5); ++spins) { if (__all(flag[9 + min(lo3, 6)] != 0u)) break; }
      asm volatile("" ::: "memory");
      SB();
      const LAS v4u* XR = (const LAS v4u*)(L + L_H + (lane >> 5) * 1024);
      const bool in = (lane & 31) < 28;
      const v4u xa = XR[in ? 2 * (lane & 31) : 0], xb = XR[in ? 2 * (lane & 31) + 1 : 0];
      const float pv = bflo(*(const LAS unsigned*)XR);
      v2f s1 = {0.f, 0.f}, s2 = {0.f, 0.f};
#pragma unroll
      for (int k = 0; k < 8; ++k) { const unsigned u = k < 4 ? xa[k & 3] : xb[k & 3]; const v2f d = v2f{bflo(u), bfhi(u)} - pv; s1 += d; s2 += d * d; }
      float q = tr16(in ? s1.x + s1.y : 0.f, in ? s2.x + s2.y : 0.f);
      q += dpp<ROR8>(q); q += dpp<ROR4>(q); q += dpp<XOR2>(q); q += dpp<XOR1>(q);
      q *= 1.f / D;
      const auto qq = __builtin_amdgcn_permlane16_swap(__float_as_uint(q), __float_as_uint(q), false, false);
      const float m1 = __uint_as_float(qq[0]), m2 = __uint_as_float(qq[1]);
      const float rs = __builtin_amdgcn_rcpf(rbf(__builtin_amdgcn_sqrtf(rbf(rbf(fmaxf(m2 - m1 * m1, 0.f)) + 1e-5f)))), mr = rbf(pv + m1) * rs;
      const auto r2 = __builtin_amdgcn_permlane32_swap(__float_as_uint(rs), __float_as_uint(rs), false, false);
      const auto mm = __builtin_amdgcn_permlane32_swap(__float_as_uint(mr), __float_as_uint(mr), false, false);
      float r0 = __uint_as_float(r2[0]), r1 = __uint_as_float(r2[1]);
      const float mr0 = __uint_as_float(mm[0]), mr1 = __uint_as_float(mm[1]);
      v4f off4 = {cg_.z - mr0 * cg_.x, cg_.w - mr1 * cg_.y, cu_.z - mr0 * cu_.x, cu_.w - mr1 * cu_.y};
      LAS u16* aw = (LAS u16*)(L + L_ACT + w * 256 + lo3 * 16 + hi3 * 2);
      const LAS v4u* E = (const LAS v4u*)(L + L_ACT + w * 256 + (lane & 8) * 16 + g4 * 16);
      const LAS v4f* GW = (const LAS v4f*)(L + L_GU + lo16);
      PIN(off4); PIN(r0); PIN(r1); PIN(aw); PIN(E); PIN(GW);
      wgbar();
      v4f gw[7];
#pragma unroll
      for (int k = 0; k < 7; ++k) gw[k] = GW[k * 64];
      SB();
      const v4f a = (((gw[0] + gw[1]) + (gw[2] + gw[3])) + ((gw[4] + gw[5]) + gw[6])) * v4f{r0, r1, r0, r1} + off4;
      const unsigned gb = pk2(a.x, a.y), ub = pk2(a.z, a.w), sg = pk2(silu(bflo(gb)), silu(bfhi(gb)));
      const unsigned ac = pk2(bflo(sg) * bflo(ub), bfhi(sg) * bfhi(ub));
      aw[0] = (u16)ac; aw[64] = (u16)(ac >> 16);
      asm volatile("" ::: "memory");
      const v4u e0 = E[0], e1 = E[4];
      publish_tile(ws, tile_gemv<true>(wc0, e0, wc1, e1), so, so2, tag);
      STAMP(16);
      asm volatile(".Lmk_end2:");
    } else if (j < 7) {
      int of[4]; v2u gg[4];
#pragma unroll
      for (int k = 0; k < 4; ++k) of[k] = y1_line(cg, k * 32, near) + t * 16;
      LAS float* rw_ = (LAS float*)(L + L_RED + (w * 16 + (lane >> 5) * 8 + lo3) * 4);
      const LAS float* rr = (const LAS float*)(L + L_RED + n16 * 4);
      float gaf = (lane & 8) ? bfhi(gav) : bflo(gav), xf = bflo((lane & 8) ? xin1 : xin0);
      int xo = (lane < 32 && !(lane & 1)) ? OFF_X1 + (lane & 16) * 512 + cg * 128 + (n16 >> 1) * 8 : OOB;
      PIN(rw_); PIN(rr); PIN(gaf); PIN(xf); PIN(xo);
      gather_all<4>(ws, of, gg, tag);
      STAMP(12);
      if (PX == 4) probeX = 2 * NOWT();
      const float a0 = (__uint_as_float(gg[0].x) + __uint_as_float(gg[1].x)) + (__uint_as_float(gg[2].x) + __uint_as_float(gg[3].x));
      const float a1 = (__uint_as_float(gg[0].y) + __uint_as_float(gg[1].y)) + (__uint_as_float(gg[2].y) + __uint_as_float(gg[3].y));
      float v2 = tr32(a0, a1); v2 = tr16(v2, v2); v2 += dpp<ROR8>(v2);
      *rw_ = v2;
      wgbar();
      if (w == 0) {
        const float x1u = xf + rbf(gaf * rbf((rr[0] + rr[16]) + (rr[32] + rr[48])));
        stsc2(ws, xo, v2u{pk2(x1u, dpp<XOR1>(x1u)), tag});
        SB();
        STAMP(13);
        const float x1o = dpp<ROR8>(x1u);
        float x1f = rbf(((lane >> 5) == (hi3 & 1)) ? x1u : x1o);
        int og[1]; v2u gv[1];
        og[0] = OFF_MOD + (5 * D + 8 * cg + (lo3 & 6)) * 8;
        gather<1, 4>(ws, og, gv, tag);
        const unsigned gbv = (lane & 1) ? gv[0].y : gv[0].x;
        float gbf = lane >= 32 ? bfhi(gbv) : bflo(gbv);
        const rsrc_t ro = mkrsrc(p.out, 2 * D * 2);
        int oo = (lane & 25) == 0 ? ((lane >> 5) * D + 8 * cg + lo3) * 2 : OOB;
        PIN(gbf); PIN(x1f); PIN(oo);
        int o2[4]; v2u g2[4];
#pragma unroll
        for (int k = 0; k < 4; ++k) o2[k] = (k * 8 + hi3 < N2) ? y2_line(cg, 0, near) + k * 1024 + lo16 : OOB;
        gather_all<4>(ws, o2, g2, tag, 0xffffffff00000000ull);
        STAMP(17);
        const float c0 = (__uint_as_float(g2[0].x) + __uint_as_float(g2[1].x)) + (__uint_as_float(g2[2].x) + __uint_as_float(g2[3].x));
        const float c1 = (__uint_as_float(g2[0].y) + __uint_as_float(g2[1].y)) + (__uint_as_float(g2[2].y) + __uint_as_float(g2[3].y));
        float y2 = tr32(c0, c1); y2 = tr16(y2, y2); y2 += dpp<ROR8>(y2);
        const float res = x1f + rbf(gbf * rbf(y2));
        if (PX == 1) probeX = 10 * (int)(tk0 - tent);
        if (PX == 2) probeX = 10 * (int)(tcom - tk0);
        if (PX == 7) probeX = 2 * NOWT();
        while (NOWT() < 4000 + probeX) { }
        __builtin_amdgcn_raw_buffer_store_b32(pk2(res, dpp<XOR1>(res)), ro, oo, 0, 0);
        if (cg == 0 && lane == 0) __hip_atomic_store((GAS unsigned*)(p.ws + OFF_EPOCH), tag, __ATOMIC_RELAXED, __HIP_MEMORY_SCOPE_AGENT);
        STAMP(18);
        asm volatile(".Lmk_end3:");
      }
    }
    if (j == 8 && x < 7) {
      const int v1 = (128 * w + 8 * hi3) * pitch + (35 + x) * 128 + lo3 * 16, v2 = w < 3 ? v1 + 64 * pitch : OOB;
#pragma unroll
      for (int r = 0; r < 16; ++r) wt[r] = ldnt(rw, r < 8 ? v1 : v2, (r & 7) * pitch);
      SB();
      tile_cols(wt, wc0); tile_cols(wt + 8, wc1);
      UNIT_GEMV(35 + x, true, 8u)
      asm volatile(".Lmk_end4:");
    }
  } else {
    const int sw = w - 4;
    const size_t kvoff = ((size_t)(b * HKV + hkv) * SK + s * 512 + sw * 64) * DH;
    const rsrc_t rK = mkrsrc(p.K + kvoff, 64 * DH * 2), rV = mkrsrc(p.V + kvoff, 64 * DH * 2);
    const int vk = lo3 * 512 + (lane & 8) * 8 + g4 * 16;
    const int vv = g4 * 512 + n16 * 16;
    v4u kv[64];
    const bool heavy = j < 16;
    unsigned tau = heavy ? (TEND - P0H) * 16 / 630 : (TEND - P0L) * 16 / 630;
    const unsigned tauf = tau * 100 / (100 + CATCH);
    unsigned due = KARG * 16 / 10, lim;
    int applied = 0;
    const bool oddx = __builtin_amdgcn_s_getreg(20 | (3 << 11)) & 1;
    const bool thin = THIN == 1 ? !heavy : THIN == 2 ? !oddx : THIN == 3 ? (!heavy && !oddx) : THIN == 4 ? !(heavy && oddx) : false;
#define GRP(i) ((i) < 32 ? KB : VB)
#define PACE(i) if ((i) % GRP(i) == 0) { if (FB) { ALIGN() } pace(due, lim, GRP(i) * tau, GRP(i) * tauf, BURST * tauf, tk0); \
      if (FB) *(volatile LAS int*)(L + L_DUE + sw * 4) = (i) + GRP(i) < 64 ? (int)(due + GRP(i) * tau) - applied : 0x7fffffff; }
    const int rot = ROT == 1 ? sw + j : ROT == 2 ? j : ROT == 3 ? j >> 3 : 0;
    const int mix = MIX == 1 ? (sw & 1) * 8192 : MIX == 2 ? (sw & 7) * 4096 : MIX == 3 ? ((sw + j) & 7) * 4096 : MIX == 4 ? (j & 7) * 4096 : MIX == 5 ? (j & 1) * 16384 : MIX == 6 ? (sw & 1) * 4096 + (sw & 2) * 8192 : MIX == 7 ? (sw & 1) * 16384 : MIX == 8 ? (j & 1) * 16384 + (j & 2) * 2048 : MIX == 9 ? (j & 1) * 16384 + (j & 2) * 4096 : 0;
#define KSEG(st) ((((st) + rot * KB) & 3) * 128)
#define VSEG(i) ((((i) + rot) & 1) * 256)
#define ALIGN() if (TBASE) { const int c_ = __builtin_amdgcn_readfirstlane(*(volatile LAS int*)(L + L_VF)); due += c_ - applied; applied = c_; }
#define PARK(i) ((i) < 4 ? L_V + sw * 4096 + (i) * 1024 : L_W + (56 + sw * 7 + (i) - 4) * 1024)
#define LOADKV(i) kv[i] = (i) < 32 ? ldnt(rK, vk, ((((i) >> 2) * 4096) ^ mix) + KSEG(i)) : ldnt(rV, vv, (((((i) - 32) >> 1) * 2048) ^ mix) + VSEG(i))
#define ISSUE_TO(FROM, TO) _Pragma("unroll") for (int i_ = (FROM); i_ < (TO); ++i_) { PACE(i_) LOADKV(i_); SB(); }
#define LATE(i)
#pragma unroll
    for (int i = 0; i < FIRSTH; ++i) { LOADKV(i); SB(); }
    due += (unsigned)((heavy ? P0H : P0L) * 16 / 10) + ((heavy ? FIRSTH : FIRST) - KB) * tau + (STAG == 1 ? sw : STAG == 2 ? (j & 7) : 0) * (KB * tau >> 3); lim = due;
    if (j == 31 && sw == 7) {
      unsigned ep;
      asm volatile("s_load_dword %0, %1, 0x0\n\ts_waitcnt lgkmcnt(0)" : "=s"(ep) : "s"(p.ws) : "memory");
      const unsigned tg = ep + 1u ? ep + 1u : 1u;
      unsigned ta = tk0;
      if (LAND) { waitvm<0>(); const unsigned tl = (unsigned)__builtin_amdgcn_s_memrealtime() - LAND / 10; if ((int)(tl - ta) > 0) ta = tl; STAMP(21); }
      stsc(mkrsrc(p.ws, WS_BYTES), lane == 0 ? OFF_T0 + x * 128 : OOB, v4u{ta, tg, ta, tg});
      SB();
    }
    if (FB) *(volatile LAS int*)(L + L_DUE + sw * 4) = (int)(due + KB * tau);
    if (heavy) wgbar();
#pragma unroll
    for (int i = FIRSTH; i < FIRST; ++i) { if (heavy) PACE(i) LOADKV(i); SB(); }
    if (!heavy) wgbar();
#pragma unroll
    for (int i = FIRST; i < 32; ++i) {
      if (!FB && (i == 2 || i == 4 || i == 6 || i == 10 || i == 12 || i == 14 || i == 18)) ALIGN()
      waitvm<TD - 1>(); PACE(i) LATE(i) LOADKV(i); SB();
    }
    {
      const unsigned lb = (unsigned)(size_t)L;
#pragma unroll
      for (int i = 0; i < NVL; ++i) {
        waitvm<TD - 1>();
        PACE(32 + i)
        unsigned keep;
        asm volatile("s_mov_b32 %0, m0\n\ts_mov_b32 m0, %1\n\ts_nop 0\n\tbuffer_load_dwordx4 %2, %3, %4 offen nt lds\n\ts_mov_b32 m0, %0"
                     : "=&s"(keep) : "s"(lb + PARK(i)), "v"(vv), "s"(rV), "s"((((i >> 1) * 2048) ^ mix) + VSEG(i)) : "memory");
        SB();
      }
    }
    STAMP(5);
    lds_wait_ge(flag, 1u);
    v4u qa[4];
#pragma unroll
    for (int st = 0; st < 4; ++st) qa[st] = *(const LAS v4u*)(L + L_Q + (lane & 1) * 512 + KSEG(st) + (lane & 2) * 32 + g4 * 16);
    ALIGN()
    STAMP(6);
    float ev0 = 0.f, ev1 = 0.f, ov0 = 0.f, ov1 = 0.f;
    const bool c2 = lane & 8;
#pragma unroll
    for (int tt = 0; tt < 8; ++tt) {
      v4f a = Z4;
#pragma unroll
      for (int st = 0; st < 4; ++st) {
        const int n = tt * 4 + st;
        ISSUE_TO(n == 0 ? 32 + NVL : issued_before(n - 1), issued_before(n))
        a = mfma(qa[st], kv[n], a);
        SB();
      }
      const float v0 = c2 ? a.z : a.x, v1 = c2 ? a.w : a.y;
      const bool mine = g4 == (tt >> 1);
      if (tt & 1) { ov0 = mine ? v0 : ov0; ov1 = mine ? v1 : ov1; } else { ev0 = mine ? v0 : ev0; ev1 = mine ? v1 : ev1; }
    }
    const float sc0 = rbf(trd<ROR8>(ev0, ov0, c2)) * (0.0625f * 1.4426950408889634f);
    const float sc1 = rbf(trd<ROR8>(ev1, ov1, c2)) * (0.0625f * 1.4426950408889634f);
    {
      auto sx = __builtin_amdgcn_permlane32_swap(__float_as_uint(sc0), __float_as_uint(sc1), false, false);
      float m = fmaxf(__uint_as_float(sx[0]), __uint_as_float(sx[1]));
      m = max16(m); m = fmaxf(m, dpp<ROR8>(m)); m = fmaxf(m, dpp<ROR4>(m)); m = fmaxf(m, dpp<XOR2>(m)); m = fmaxf(m, dpp<XOR1>(m));
      const float m0 = __builtin_bit_cast(float, __builtin_amdgcn_readlane(__builtin_bit_cast(int, m), 0));
      const float m1 = __builtin_bit_cast(float, __builtin_amdgcn_readlane(__builtin_bit_cast(int, m), 32));
      const float p0 = __builtin_amdgcn_exp2f(sc0 - m0), p1 = __builtin_amdgcn_exp2f(sc1 - m1);
      float l = tr32(p0, p1);
      l = tr16(l, l); l += dpp<ROR8>(l); l += dpp<ROR4>(l); l += dpp<XOR2>(l); l += dpp<XOR1>(l);
      const unsigned pp = pk2(p0, p1);
      LAS unsigned* P = (LAS unsigned*)(L + L_P + sw * 1024 + (lane & 3) * 64 + (lane >> 2) * 4);
      P[0] = pp & 0xffffu; P[64] = pp << 16; P[128] = pp >> 16; P[192] = pp & 0xffff0000u;
      if ((lane & 31) == 0) *(LAS v2f*)(L + L_ML + ((lane >> 5) * 8 + sw) * 8) = v2f{m, l};
    }
    lds_inc(L, 3, lane);
    waitvm<issued_before(31) - 32 - NVL>();
#pragma unroll
    for (int i = 0; i < NVL; ++i) kv[32 + i] = *(const LAS v4u*)(L + PARK(i) + lo16);
    v4u pr[4];
#pragma unroll
    for (int k = 0; k < 4; ++k) pr[k] = *(const LAS v4u*)(L + L_P + sw * 1024 + (lane & 8) * 64 + (lane & 1) * 256 + g4 * 64 + k * 16);
    STAMP(7);
    const rsrc_t ws = mkrsrc(p.ws, WS_BYTES);
    int om[2];
    om[0] = (roleF && sw < 7) ? OFF_MOD + (3 * D + 64 * sw + 8 * hi3 + 2 * (lane & 3)) * 8 : OOB; om[1] = (roleF && sw < 7) ? om[0] + D * 8 : OOB;
    const v4u ms = ldsc(ws, om[0]), mc = ldsc(ws, om[1]);
    SB();
    v4f acc0 = Z4, acc1 = Z4;
    const int jq = (lane >> 1) & 3;
#pragma unroll
    for (int ip = 0; ip < 16; ++ip) {
      ISSUE_TO(issued_before(32 + 2 * ip - 1), issued_before(32 + 2 * ip + 1))
      if (ip == 5 || ip == 9 || ip == 13) STAMP(ip == 5 ? 24 : ip == 9 ? 27 : 29);
      if (ip == ML_AT) lds_inc(L, 6, lane);
      if (issued_before(32 + 2 * ip - 1) < 64 && issued_before(32 + 2 * ip + 1) == 64) lds_inc(L, 4, lane);
      const unsigned pu = pr[ip >> 2][ip & 3];
      const v4u pa = {jq == 0 ? pu : 0u, jq == 1 ? pu : 0u, jq == 2 ? pu : 0u, jq == 3 ? pu : 0u};
      acc0 = mfma(pa, kv[32 + 2 * ip], acc0); acc1 = mfma(pa, kv[33 + 2 * ip], acc1);
      SB();
    }
    LAS v4f* O = (LAS v4f*)(L + L_V + sw * 4096 + (g4 >> 1) * 1024 + n16 * 32 + (g4 & 1) * 16);
    O[VSEG(0) / 8] = acc0; O[VSEG(1) / 8] = acc1;
    STAMP(8);
    wgbar();
    if (roleF && sw < 7) {
      const unsigned tag = __builtin_amdgcn_readfirstlane(flag[1]);
      const bool hb = lane & 4;
      v2u gm[2] = {{ms.x, ms.z}, {mc.x, mc.z}};
      if (!__all((ms.y == tag) & (ms.w == tag) & (mc.y == tag) & (mc.w == tag))) gather<2>(ws, om, gm, tag);
      const unsigned cbp = hb ? phi(gm[1].x, gm[1].y) : plo(gm[1].x, gm[1].y), cb1 = pk2(1.f + bflo(cbp), 1.f + bfhi(cbp));
      LAS unsigned* Z = (LAS unsigned*)(L + L_V + sw * 4096 + 3072 + (lane & 4) * 32 + hi3 * 16 + (lane & 3) * 4);
      const LAS v4u* ZA = (const LAS v4u*)(L + L_V + sw * 4096 + 3072 + (lane & 3) * 128 + g4 * 16);
      Z[0] = cb1; Z[64] = hb ? phi(gm[0].x, gm[0].y) : plo(gm[0].x, gm[0].y);
      v4u wt[8], gc0[8], gc1[8];
      v4f y[8];
#pragma unroll
      for (int k = 0; k < 8; ++k) wt[k] = *(const LAS v4u*)(L + L_W + (sw * 8 + k) * 1024 + lo16);
      tile_cols(wt, gc0);
      {
        const v4u h0 = ZA[0], h1 = ZA[4];
#pragma unroll
        for (int c = 0; c < 8; ++c) { y[c] = mfma(h0, gc0[c], Z4); PIN(y[c]); }
        wgbar();
#pragma unroll
        for (int k = 0; k < 8; ++k) wt[k] = *(const LAS v4u*)(L + L_W + (56 + sw * 8 + k) * 1024 + lo16);
        tile_cols(wt, gc1);
#pragma unroll
        for (int c = 0; c < 8; ++c) y[c] = mfma(h1, gc1[c], y[c]);
        if (lane < 16) {
          LAS v4f* G = (LAS v4f*)(L + L_V + sw * 4096 + 1024 + lo3 * 32 + (lane & 8) * 2);
#pragma unroll
          for (int c = 0; c < 8; ++c) G[16 * c] = y[c];
        }
      }
      float cl = bflo(cb1), ch = bfhi(cb1);
      int ox = OFF_X1 + ((__builtin_amdgcn_readfirstlane(flag[8]) >> x) & 1) * 8192 + (8 * sw + hi3) * 128 + lo3 * 8;
      LAS v2f* GO = (LAS v2f*)(L + L_GU + (lane < 16 ? sw : 7) * 1024 + lo3 * 16 + (lane & 8));
      LAS unsigned* XC = (LAS unsigned*)(L + L_H + (lane & 4) * 256 + sw * 128 + hi3 * 16 + (lane & 3) * 4);
      PIN(cl); PIN(ch); PIN(ox); PIN(XC); PIN(GO);
      lds_inc(L, 2, lane);
      STAMP(28);
      const unsigned xg = gather8(ws, ox, tag);
      STAMP(15);
      const float x0 = bflo(xg), x1 = bfhi(xg), z0 = x0 * cl, z1 = x1 * ch;
      const unsigned zh = pk2(z0, z1);
      Z[0] = zh; Z[64] = pk2(z0 - bflo(zh), z1 - bfhi(zh));
      asm volatile("" ::: "memory");
      const v4u h0 = ZA[0], h1 = ZA[4];
      asm volatile("" ::: "memory");
      *XC = xg;
      asm volatile("" ::: "memory");
      flag[9 + sw] = 1u;
      SB();
#pragma unroll
      for (int c = 0; c < 8; ++c) y[c] = mfma(h0, gc0[c], Z4);
      SB();
#pragma unroll
      for (int c = 0; c < 8; ++c) y[c] = mfma(h1, gc1[c], y[c]);
      if (lane < 16) {
#pragma unroll
        for (int c = 0; c < 8; ++c) GO[16 * c] = v2f{y[c].x + y[c].z, y[c].y + y[c].w};
      }
      wgbar();
    }
  }
  DIAG_FLUSH();
}

extern "C" void kernel_launch(void* const* d_in, const int* in_sizes, int n_in, void* d_out, int out_size, void* d_ws, size_t ws_size, hipStream_t stream) {
  Params p;
  p.x = (const u16*)d_in[0]; p.K = (const u16*)d_in[1]; p.V = (const u16*)d_in[2]; p.wq = (const u16*)d_in[3]; p.wo = (const u16*)d_in[4];
  p.wg = (const u16*)d_in[6]; p.wu = (const u16*)d_in[7]; p.wd = (const u16*)d_in[8]; p.c = (const u16*)d_in[9]; p.wmod = (const u16*)d_in[10];
  p.out = (u16*)d_out; p.ws = (unsigned char*)(((uintptr_t)d_ws + (2u << 20) - 1) & ~(uintptr_t)((2u << 20) - 1));
  (void)hipFuncSetAttribute((const void*)mk_kernel, hipFuncAttributeMaxDynamicSharedMemorySize, LDS_BYTES);
  hipLaunchKernelGGL(mk_kernel, dim3(NWG), dim3(NT), LDS_BYTES, stream, p);
}
```

```cpp
#include <hip/hip_runtime.h>
#include <hip/hip_bf16.h>
#include <cstdint>

typedef unsigned short u16;
typedef unsigned long long u64;
typedef unsigned v4u __attribute__((ext_vector_type(4)));
typedef unsigned v2u __attribute__((ext_vector_type(2)));
typedef float v4f __attribute__((ext_vector_type(4)));
typedef float v2f __attribute__((ext_vector_type(2)));
typedef __bf16 bf16x2 __attribute__((ext_vector_type(2)));
#define LAS __attribute__((address_space(3)))
#define GAS __attribute__((address_space(1)))
typedef __amdgpu_buffer_rsrc_t rsrc_t;

constexpr int D = 448, HQ = 16, HKV = 8, SK = 8192, DH = 256, FF = 1792;
constexpr int NWG = 256, NT = 768;
constexpr int N2 = 28;
constexpr int NCG = 56;
constexpr int OFF_EPOCH = 0;
constexpr int OFF_PREF = 512;
constexpr int OFF_MOD = 1024;
constexpr int OFF_Q = 24576;
constexpr int OFF_T0 = 57344;
constexpr int OFF_ML = 65536;
constexpr int OFF_PRB = 98304;
constexpr int OFF_X1 = 114688;
constexpr int OFF_Y1 = 131072;
constexpr int OFF_Y2 = OFF_Y1 + 56 * 16384;
constexpr int OFF_PO = 2 << 20;
constexpr int OFF_PRB1 = OFF_PO + 64 * 16384;
constexpr int WS_BYTES = OFF_PRB1 + 16384;
static_assert(OFF_Y2 + 16 * 16384 <= OFF_PO, "workspace map");
__device__ __forceinline__ int po_base(int e, unsigned near) { return OFF_PO + (e & 63) * 16384 + ((near >> (8 + (e >> 4))) & 1) * 8192; }
__device__ __forceinline__ int y1_line(int cg, int e, unsigned near) { return OFF_Y1 + (((cg >> 3) * 4 + (cg & 3)) * 2 + (e >> 6)) * 16384 + ((near >> (cg & 7)) & 1) * 8192 + (e & 63) * 128; }
__device__ __forceinline__ int y2_line(int cg, int f, unsigned near) { return OFF_Y2 + ((cg >> 4) * 4 + (cg & 3)) * 16384 + ((near >> (cg & 7)) & 1) * 8192 + ((cg >> 3) & 1) * 4096 + f * 128; }
constexpr int AUX_NT = 2, AUX_SC1 = 16;
constexpr unsigned SPIN_CAP = 1u << 20;
constexpr int OOB = 0x7ffffff0;

constexpr int TEND = 18250;
constexpr int P0H = 2300;
constexpr int COLDX = 450;
constexpr int COLDT = 560;
constexpr int TT0 = 2100;
constexpr int TMOD = 6200;
constexpr int TQ = 9500;
constexpr int TT1 = 900;
constexpr int KB = 2;
constexpr int TD = 4, WIN = 5;
constexpr int NVL = 6;
constexpr int ML_AT = 10;
constexpr int QDIV = 6, NGT = 7, NDF = 10, NPROBE = 4, PSLEEP = 8;
constexpr int NOSHIFT = -0x40000000;

constexpr int L_FLAG = 0;
constexpr int L_Q = 64;
constexpr int L_VF = 1152;
constexpr int L_ML = 1280;
constexpr int L_FW = 1408;
constexpr int L_S1 = 1664;
constexpr int L_RED = 1792;
constexpr int L_H = 2048;
constexpr int L_ACT = 4096;
constexpr int L_P = 8192;
constexpr int L_GU = L_P;
constexpr int L_V = 16384;
constexpr int L_W = 49152;
constexpr int LDS_BYTES = L_W + 112 * 1024;
static_assert(NVL <= 10 && 2 * ML_AT >= NVL && LDS_BYTES <= 160 * 1024, "LDS map");

__device__ __forceinline__ float bflo(unsigned v) { return __uint_as_float(v << 16); }
__device__ __forceinline__ float bfhi(unsigned v) { return __uint_as_float(v & 0xffff0000u); }
__device__ __forceinline__ unsigned pk2(float lo, float hi) { bf16x2 v = {(__bf16)lo, (__bf16)hi}; return __builtin_bit_cast(unsigned, v); }
__device__ __forceinline__ float rbf(float f) { return (float)(__bf16)f; }
__device__ __forceinline__ unsigned plo(unsigned a, unsigned b) { return __builtin_amdgcn_perm(b, a, 0x05040100u); }
__device__ __forceinline__ unsigned phi(unsigned a, unsigned b) { return __builtin_amdgcn_perm(b, a, 0x07060302u); }
__device__ __forceinline__ float silu_div(float x) { return x / (1.f + __expf(-x)); }
__device__ __forceinline__ float silu(float x) { return x * __builtin_amdgcn_rcpf(1.f + __builtin_amdgcn_exp2f(x * -1.4426950408889634f)); }
template <int CTRL> __device__ __forceinline__ float dpp(float x) {
  return __builtin_bit_cast(float, __builtin_amdgcn_mov_dpp(__builtin_bit_cast(int, x), CTRL, 0xf, 0xf, true));
}
constexpr int XOR1 = 0xB1, XOR2 = 0x4E, XOR7 = 0x141, ROR8 = 0x128, ROR4 = 0x124;
__device__ __forceinline__ float tr32(float a, float b) {
  auto s = __builtin_amdgcn_permlane32_swap(__float_as_uint(a), __float_as_uint(b), false, false);
  return __uint_as_float(s[0]) + __uint_as_float(s[1]);
}
__device__ __forceinline__ float tr16(float a, float b) {
  auto s = __builtin_amdgcn_permlane16_swap(__float_as_uint(a), __float_as_uint(b), false, false);
  return __uint_as_float(s[0]) + __uint_as_float(s[1]);
}
template <int CTRL> __device__ __forceinline__ float trd(float a, float b, bool bit) {
  const float keep = bit ? b : a, send = bit ? a : b;
  return keep + dpp<CTRL>(send);
}
__device__ __forceinline__ float max16(float x) {
  auto s = __builtin_amdgcn_permlane16_swap(__float_as_uint(x), __float_as_uint(x), false, false);
  return fmaxf(__uint_as_float(s[0]), __uint_as_float(s[1]));
}
__device__ __forceinline__ float wave_sum(float x) {
  x += dpp<XOR1>(x); x += dpp<XOR2>(x); x += dpp<ROR4>(x); x += dpp<ROR8>(x); x = tr16(x, x); return tr32(x, x);
}
__device__ __forceinline__ rsrc_t mkrsrc(const void* p, int bytes) { return __builtin_amdgcn_make_buffer_rsrc((void*)p, 0, bytes, 0x00020000); }
__device__ __forceinline__ v4u ldnt(rsrc_t r, int voff, int soff) { return __builtin_amdgcn_raw_buffer_load_b128(r, voff, soff, AUX_NT); }
__device__ __forceinline__ v4u ldsc(rsrc_t r, int voff) { return __builtin_amdgcn_raw_buffer_load_b128(r, voff, 0, AUX_SC1); }
__device__ __forceinline__ v2u ldsc2(rsrc_t r, int voff) { return __builtin_amdgcn_raw_buffer_load_b64(r, voff, 0, AUX_SC1); }
__device__ __forceinline__ void stsc(rsrc_t r, int voff, v4u v) { __builtin_amdgcn_raw_buffer_store_b128(v, r, voff, 0, AUX_SC1); }
__device__ __forceinline__ void stsc2(rsrc_t r, int voff, v2u v) { __builtin_amdgcn_raw_buffer_store_b64(v, r, voff, 0, AUX_SC1); }
template <int N> __device__ __forceinline__ void waitvm() { asm volatile("s_waitcnt vmcnt(%0)" :: "i"(N) : "memory"); }
#define SB() __builtin_amdgcn_sched_barrier(0)
#define PIN(x) asm volatile("" : "+v"(x))
__device__ __forceinline__ void ldswait() { asm volatile("s_waitcnt lgkmcnt(0)" ::: "memory"); }
__device__ __forceinline__ void wgbar() { SB(); asm volatile("s_waitcnt lgkmcnt(0)" ::: "memory"); __builtin_amdgcn_s_barrier(); asm volatile("" ::: "memory"); SB(); }

template <int N, int SLP = 0> __device__ __forceinline__ void gather(rsrc_t ws, int (&off)[N], v2u (&val)[N], unsigned tag) {
#pragma unroll
  for (int k = 0; k < N; ++k) val[k] = v2u{0u, 0u};
#pragma unroll 1
  for (unsigned spins = 0; spins < SPIN_CAP; ++spins) {
    v4u g[N];
#pragma unroll
    for (int k = 0; k < N; ++k) g[k] = ldsc(ws, off[k]);
    bool open = false;
#pragma unroll
    for (int k = 0; k < N; ++k) {
      const bool m = (g[k].y == tag) & (g[k].w == tag);
      val[k].x = m ? g[k].x : val[k].x; val[k].y = m ? g[k].z : val[k].y; off[k] = m ? OOB : off[k];
      open |= off[k] != OOB;
    }
    if (!__any(open)) break;
    if (SLP) __builtin_amdgcn_s_sleep(SLP);
    asm volatile("" ::: "memory");
  }
}
template <int N> __device__ __forceinline__ void gather_all(rsrc_t ws, const int (&off)[N], v2u (&val)[N], unsigned tag, u64 skip = 0) {
  unsigned spins = SPIN_CAP;
  u64 ok;
#pragma unroll 1
  do {
    asm volatile("" ::: "memory");
    v4u g[N];
#pragma unroll
    for (int k = 0; k < N; ++k) g[k] = ldsc(ws, off[k]);
    ok = --spins ? 0ull : ~0ull;
    u64 a = ~0ull;
#pragma unroll
    for (int k = 0; k < N; ++k) {
      const u64 m = __builtin_amdgcn_ballot_w64(g[k].y == tag) & __builtin_amdgcn_ballot_w64(g[k].w == tag);
      a &= k == N - 1 ? m | skip : m;
      val[k] = v2u{g[k].x, g[k].z};
    }
    ok |= a;
  } while (ok != ~0ull);
}
__device__ __forceinline__ unsigned gather8(rsrc_t ws, int off, unsigned tag) {
  unsigned spins = SPIN_CAP;
  u64 ok; v2u g;
#pragma unroll 1
  do {
    asm volatile("" ::: "memory");
    g = ldsc2(ws, off);
    ok = --spins ? 0ull : ~0ull;
    ok |= __builtin_amdgcn_ballot_w64(g.y == tag);
  } while (ok != ~0ull);
  return g.x;
}
__device__ __forceinline__ void lds_wait_ge(volatile LAS unsigned* word, unsigned want) {
#pragma unroll 1
  for (unsigned spins = 0; spins < (SPIN_CAP << 4); ++spins) { if (*word >= want) break; __builtin_amdgcn_s_sleep(1); }
  asm volatile("" ::: "memory");
  SB();
}
typedef __bf16 v8bf __attribute__((ext_vector_type(8)));
__device__ __forceinline__ v4f mfma(v4u a, v4u b, v4f c) {
  return __builtin_amdgcn_mfma_f32_16x16x32_bf16(__builtin_bit_cast(v8bf, a), __builtin_bit_cast(v8bf, b), c, 0, 0, 0);
}
__device__ __forceinline__ void tile_cols(const v4u* src, v4u (&wc)[8]) {
#pragma unroll
  for (int k = 0; k < 4; ++k) {
    wc[2 * k] = v4u{plo(src[0][k], src[1][k]), plo(src[2][k], src[3][k]), plo(src[4][k], src[5][k]), plo(src[6][k], src[7][k])};
    wc[2 * k + 1] = v4u{phi(src[0][k], src[1][k]), phi(src[2][k], src[3][k]), phi(src[4][k], src[5][k]), phi(src[6][k], src[7][k])};
    PIN(wc[2 * k]); PIN(wc[2 * k + 1]);
  }
}
template <bool TWO> __device__ __forceinline__ v4f tile_gemv(const v4u (&w0)[8], v4u in0, const v4u (&w1)[8], v4u in1) {
  v4f d[8], r;
#pragma unroll
  for (int c = 0; c < 10; ++c) {
    if (c < 8) { d[c] = mfma(w0[c], in0, v4f{0.f, 0.f, 0.f, 0.f}); if (TWO) d[c] = mfma(w1[c], in1, d[c]); }
    if (c == 2) r = d[0];
    if (c > 2) {
      const bool m = __builtin_amdgcn_inverse_ballot_w64(0x0101010101010101ull << (c - 2));
      r = v4f{m ? d[c - 2].x : r.x, m ? d[c - 2].y : r.y, m ? d[c - 2].z : r.z, m ? d[c - 2].w : r.w};
      PIN(r);
    }
    SB();
  }
  return r;
}
__device__ __forceinline__ void publish_tile(rsrc_t ws, v4f y, int so, int so2, unsigned tag) {
  const bool hi = __builtin_amdgcn_inverse_ballot_w64(0xff00ff00ff00ff00ull);
  const float r0 = dpp<ROR8>(hi ? y.x : y.y), r1 = dpp<ROR8>(hi ? y.z : y.w);
  __builtin_amdgcn_raw_buffer_store_b128(v4u{__float_as_uint(hi ? r0 : y.x), tag, __float_as_uint(hi ? y.y : r0), tag}, ws, so, 0, AUX_SC1);
  __builtin_amdgcn_raw_buffer_store_b128(v4u{__float_as_uint(hi ? r1 : y.z), tag, __float_as_uint(hi ? y.w : r1), tag}, ws, so2, 0, AUX_SC1);
}

struct Params {
  const u16 *x, *K, *V, *wq, *wo, *wg, *wu, *wd, *c, *wmod;
  u16* out;
  unsigned char* ws;
};

constexpr int issued_before(int n) {
  int a = (32 + NVL + KB + (n < 32 ? n : 31) / QDIV) / KB * KB, b = (n + WIN) / KB * KB;
  int m = a > b ? a : b;
  return m > 64 ? 64 : m;
}
__device__ __forceinline__ void lds_spin_ge(volatile LAS unsigned* word, unsigned want) {
#pragma unroll 1
  for (unsigned spins = 0; spins < (SPIN_CAP << 5); ++spins) { if (*word >= want) break; }
  asm volatile("" ::: "memory");
  SB();
}
__device__ __forceinline__ void pace(unsigned& due, unsigned step, unsigned t0) {
  asm volatile("s_add_u32 %0, %0, %1\n"
               ".Lpw%=:\n\ts_memrealtime vcc\n\ts_waitcnt lgkmcnt(0)\n\ts_sub_u32 vcc_lo, vcc_lo, %2\n\ts_lshl_b32 vcc_lo, vcc_lo, 4\n\t"
               "s_cmp_lt_i32 vcc_lo, %0\n\ts_cbranch_scc1 .Lpw%="
               : "+s"(due) : "s"(step), "s"(t0) : "vcc", "scc", "memory");
}
__device__ __forceinline__ unsigned read_tag(const unsigned char* ws) {
  unsigned ep;
  asm volatile("s_load_dword %0, %1, 0x0\n\ts_waitcnt lgkmcnt(0)" : "=s"(ep) : "s"(ws) : "memory");
  return ep + 1u ? ep + 1u : 1u;
}
__device__ __forceinline__ void load_to_lds(unsigned dst, const u16* src) {
  unsigned keep;
  asm volatile("s_mov_b32 %0, m0\n\ts_mov_b32 m0, %1\n\ts_nop 0\n\tglobal_load_lds_dwordx4 %2, off nt\n\ts_mov_b32 m0, %0" : "=&s"(keep) : "s"(dst), "v"(src) : "memory");
}
__device__ __forceinline__ void wait_until(unsigned due) {
  while ((int)((unsigned)__builtin_amdgcn_s_memrealtime() - due) < 0) __builtin_amdgcn_s_sleep(1);
  SB();
}
__device__ __forceinline__ void lds_inc(LAS unsigned char* L, int word, int lane) {
  ldswait();
  if (lane == 0) __hip_atomic_fetch_add((LAS unsigned*)(L + L_FLAG + word * 4), 1u, __ATOMIC_RELAXED, __HIP_MEMORY_SCOPE_WORKGROUP);
  SB();
}
constexpr v4f Z4 = {0.f, 0.f, 0.f, 0.f};

extern "C" __global__ void __launch_bounds__(NT, 3) mk_kernel(Params p) {
  extern __shared__ __attribute__((aligned(16))) unsigned char smem[];
  LAS unsigned char* L = (LAS unsigned char*)smem;
  const int bid = blockIdx.x, t = threadIdx.x, lane = t & 63, w = __builtin_amdgcn_readfirstlane(t >> 6);
  const int x = bid & 7, j = bid >> 3;
  const int hkv = x, b = j >> 4, s = j & 15;
  const int lo16 = lane * 16, g4 = lane >> 4, n16 = lane & 15, hi3 = lane >> 3, lo3 = lane & 7;
  volatile LAS unsigned* flag = (volatile LAS unsigned*)(L + L_FLAG);
#define ROLES() \
    int jr = j, xr = x; u64 tkw; asm volatile("s_waitcnt lgkmcnt(0)\n\ts_mov_b64 %2, %3" : "+s"(jr), "+s"(xr), "=s"(tkw) : "s"(tk64)); \
    const unsigned tk0 = (unsigned)tkw; \
    const bool roleB = jr < 8;                                                 \
    const bool roleA = !roleB && jr * 8 + xr < 99;                             \
    const bool roleF = jr < 16 && jr * 8 + xr >= 100;                          \
    const int f = 3 * xr - 13 + ((0x43210000u >> (4 * xr)) & 7) + jr; \
    const int unit = jr + (roleB ? 8 * xr : 4 * xr - 8 + ((0x33333210u >> (4 * xr)) & 3));
  unsigned kvb = (unsigned)((b * HKV + hkv) * SK + s * 512 + (w - 4) * 64) * (DH * 2);
  int vk = lo3 * 512 + (lane & 8) * 8 + g4 * 16;
  asm volatile("" : "+s"(kvb)); PIN(vk);
  SB();
  u64 tk64;
  asm volatile("s_memrealtime %0" : "=s"(tk64) : "s"(p.K), "s"(p.ws) : "memory");
  SB();
  p.ws = (unsigned char*)(((uintptr_t)p.ws + (2u << 20) - 1) & ~(uintptr_t)((2u << 20) - 1));
  if (w < 4) {
    __builtin_amdgcn_s_setprio(2);
    ROLES()
    const rsrc_t ws = mkrsrc(p.ws, WS_BYTES);
    const bool roleS = j >= 16;
    const int e = x * 16 + s;
    const bool on = t < D / 2;
    LAS unsigned* H = (LAS unsigned*)(L + L_H + t * 4);
    if (t < 16) flag[t] = 0u;
    if (t == 192) *(LAS int*)(L + L_VF) = NOSHIFT;
    v4u wt[16], xr0 = {0u, 0u, 0u, 0u}, xr1 = xr0;
    unsigned xo0 = 0u, xo1 = 0u;
    if (roleA | roleB) {
      const rsrc_t rx = mkrsrc(roleB ? p.x : p.c, 2 * D * 2);
      const int xo = on ? t * 4 : OOB, xv = (roleB && lane < 56) ? lo16 : OOB;
      xo0 = __builtin_amdgcn_raw_buffer_load_b32(rx, xo, 0, 0); xo1 = __builtin_amdgcn_raw_buffer_load_b32(rx, xo, D * 2, 0);
      xr0 = __builtin_amdgcn_raw_buffer_load_b128(rx, xv, 0, 0); xr1 = __builtin_amdgcn_raw_buffer_load_b128(rx, xv, D * 2, 0);
    }
    if (j >= 16) wgbar();
    const unsigned tag = read_tag(p.ws);
    if (j < 16) wgbar();
    unsigned tcom = tk0;
    if (w == 3 || roleB) {
      int mx, cold;
#pragma unroll 1
      for (int r = 0; r < 3; ++r) {
        wait_until(tk0 + (TT0 + r * TT1) / 10);
        u64 v[8];
        asm volatile("s_load_dwordx2 %0, %8, 0\n\ts_load_dwordx2 %1, %8, 128\n\ts_load_dwordx2 %2, %8, 256\n\ts_load_dwordx2 %3, %8, 384\n\t"
                     "s_load_dwordx2 %4, %8, 512\n\ts_load_dwordx2 %5, %8, 640\n\ts_load_dwordx2 %6, %8, 768\n\ts_load_dwordx2 %7, %8, 896\n\ts_waitcnt lgkmcnt(0)"
                     : "=&s"(v[0]), "=&s"(v[1]), "=&s"(v[2]), "=&s"(v[3]), "=&s"(v[4]), "=&s"(v[5]), "=&s"(v[6]), "=&s"(v[7]) : "s"(p.ws + OFF_T0 + r * 1024) : "memory");
        mx = 0; cold = 0;
#pragma unroll
        for (int k = 0; k < 8; ++k) { const bool ok = (unsigned)(v[k] >> 32) == tag; mx = max(mx, ok ? min(300, (int)((unsigned)v[k] - tk0)) : 0); cold |= ok ? (int)v[k] & 1 : 3; }
        if (cold < 2) break;
      }
      tcom = tk0 + mx;
      if (t == 192) *(LAS int*)(L + L_VF) = mx * 16 + (cold & 1);
    }
    v4u wc0[8], wc1[8];
#define UNIT_GEMV(UNIT, ISA, CNT) { \
      ldswait(); \
      const LAS v4u* HA = (const LAS v4u*)(L + L_H + (lane & 1) * 1024 + (128 * w + 16 * g4 + (lane & 4) * 2) * 2); \
      const v4u ha0 = HA[0], ha1 = HA[8]; \
      v4f y[8]; \
_Pragma("unroll") \
      for (int c = 0; c < 8; ++c) y[c] = mfma(ha0, wc0[c], Z4); \
_Pragma("unroll") \
      for (int c = 0; c < 8; ++c) y[c] = mfma(ha1, wc1[c], y[c]); \
      if (lane < 8 || (lane >= 24 && lane < 32)) { \
        LAS v2f* G = (LAS v2f*)(L + L_W + w * 16384 + (lane & 8) * 64 + lo3 * 64); \
_Pragma("unroll") \
        for (int c = 0; c < 8; ++c) G[c] = v2f{y[c].x, y[c].y}; \
      } \
      lds_inc(L, 2, lane); \
      if (w == 0) { \
        lds_wait_ge(flag + 2, CNT); \
        const LAS v4f* G = (const LAS v4f*)(L + L_W + (lane >> 5) * 32768 + (lane & 31) * 16); \
        const v4f a = (G[0] + G[32]) + (G[1024] + G[1056]); \
        const float e0 = (ISA) ? tr32(a.x, a.z) : tr32(a.x, a.y), e1 = (ISA) ? tr32(a.y, a.w) : tr32(a.z, a.w); \
        const int so = (ISA) ? OFF_MOD + (64 * (UNIT) + 2 * (lane & 31) + (lane >> 5)) * 8 : OFF_Q + ((lane >> 5) * 2048 + (UNIT) * 32 + (lane & 31)) * 8; \
        stsc2(ws, so, v2u{pk2(e0, e1), tag}); \
        SB(); \
      } \
    }
    float n00 = 0.f, n01 = 0.f, n10 = 0.f, n11 = 0.f;
    if (roleA) {
      H[0] = pk2(silu_div(bflo(xo0)), silu_div(bfhi(xo0))); H[256] = pk2(silu_div(bflo(xo1)), silu_div(bfhi(xo1)));
    } else if (roleB) {
      float s0 = 0.f, s1 = 0.f;
#pragma unroll
      for (int k = 0; k < 4; ++k) { s0 += bflo(xr0[k]) + bfhi(xr0[k]); s1 += bflo(xr1[k]) + bfhi(xr1[k]); }
      const float m0 = wave_sum(s0) / D, m1 = wave_sum(s1) / D;
      float v0 = 0.f, v1 = 0.f;
#pragma unroll
      for (int k = 0; k < 4; ++k) {
        const float a = bflo(xr0[k]) - m0, c = bfhi(xr0[k]) - m0, d = bflo(xr1[k]) - m1, q = bfhi(xr1[k]) - m1;
        v0 += a * a + c * c; v1 += d * d + q * q;
      }
      if (lane >= 56) { v0 = 0.f; v1 = 0.f; }
      v0 = wave_sum(v0) / D; v1 = wave_sum(v1) / D;
      const float sd0 = rbf(sqrtf(rbf(rbf(v0) + 1e-5f))), sd1 = rbf(sqrtf(rbf(rbf(v1) + 1e-5f)));
      const float mb0 = rbf(m0), mb1 = rbf(m1);
      n00 = rbf(rbf(bflo(xo0) - mb0) / sd0); n01 = rbf(rbf(bfhi(xo0) - mb0) / sd0); n10 = rbf(rbf(bflo(xo1) - mb1) / sd1); n11 = rbf(rbf(bfhi(xo1) - mb1) / sd1);
    }
    PIN(n00); PIN(n01); PIN(n10); PIN(n11);
    if (roleA | roleB) {
#pragma unroll
      for (int r = 0; r < 16; ++r) wt[r] = *(const LAS v4u*)(L + L_W + (w * 16 + (w < 3 ? r : r & 7)) * 1024 + lo16);
      tile_cols(wt, wc0); tile_cols(wt + 8, wc1);
    }
    __builtin_amdgcn_s_waitcnt(0x0F70);
    waitvm<0>();
    if (roleB) {
      wait_until(tcom + TMOD / 10);
      int off[2]; v2u g[2];
      off[0] = on ? OFF_MOD + t * 16 : OOB; off[1] = on ? OFF_MOD + D * 8 + t * 16 : OOB;
      gather<2, PSLEEP>(ws, off, g, tag);
      const unsigned c0 = pk2(1.f + bflo(g[1].x), 1.f + bflo(g[1].y)), c1 = pk2(1.f + bfhi(g[1].x), 1.f + bfhi(g[1].y));
      const unsigned u0 = pk2(n00 * bflo(c0), n01 * bfhi(c0)), u1 = pk2(n10 * bflo(c1), n11 * bfhi(c1));
      H[0] = on ? pk2(bflo(u0) + bflo(g[0].x), bfhi(u0) + bflo(g[0].y)) : 0u; H[256] = on ? pk2(bflo(u1) + bfhi(g[0].x), bfhi(u1) + bfhi(g[0].y)) : 0u;
    }
    if (roleA | roleB) UNIT_GEMV(unit, roleA, 4u)
    if (w == 1) {
      u64 pc; unsigned back, l1, l2, l3;
      asm volatile("s_getpc_b64 %0\n.Lmk_anchor:\n\ts_mov_b32 %1, .Lmk_anchor-mk_kernel\n\ts_mov_b32 %2, .Lmk_end1-mk_kernel\n\ts_mov_b32 %3, .Lmk_end2-mk_kernel\n\ts_mov_b32 %4, .Lmk_end4-mk_kernel"
                   : "=s"(pc), "=s"(back), "=s"(l1), "=s"(l2), "=s"(l3));
      const unsigned len = max(l1, max(l2, l3));
      const unsigned lines = (len + 127u) >> 7, per = (lines + 31u) >> 5, li = min(j * per + min((unsigned)lane, per - 1u), lines - 1u), co = li * 128u;
      const unsigned cpf = *(const GAS unsigned*)(pc - back + co);
      asm volatile("" :: "v"(cpf));
      lds_wait_ge(flag + 6, 1u);
      const unsigned cp2 = __hip_atomic_load((const GAS unsigned*)(pc - back + co), __ATOMIC_RELAXED, __HIP_MEMORY_SCOPE_AGENT);
      asm volatile("" :: "v"(cp2));
    }
    if (w == 0 && j == 31 && x < 4) {
      __builtin_amdgcn_raw_buffer_store_b32(0u, ws, n16 < NPROBE ? OFF_PRB + (g4 & 1) * 8192 + (g4 >> 1) * (OFF_PRB1 - OFF_PRB) + x * 2048 + n16 * 128 : OOB, 0, AUX_SC1);
      waitvm<0>();
      unsigned mn[4] = {~0u, ~0u, ~0u, ~0u};
#pragma unroll 1
      for (int r = 0; r < NPROBE; ++r) {
        const int pb = __builtin_amdgcn_readfirstlane(OFF_PRB + x * 2048 + r * 128);
#pragma unroll
        for (int k = 0; k < 4; ++k) {
          u64 a, c; unsigned v;
          asm volatile("s_memtime %0\n\ts_waitcnt lgkmcnt(0)\n\ts_load_dword %2, %3, %4\n\ts_waitcnt lgkmcnt(0)\n\ts_memtime %1\n\ts_waitcnt lgkmcnt(0)"
                       : "=&s"(a), "=&s"(c), "=&s"(v) : "s"(p.ws), "s"(pb + (k & 1) * 8192 + (k >> 1) * (OFF_PRB1 - OFF_PRB)) : "memory");
          mn[k] = min(mn[k], (unsigned)(c - a));
        }
      }
      const unsigned bits = (mn[1] < mn[0]) | (mn[3] < mn[2]) << 1;
      stsc(ws, lane == 0 ? OFF_PREF + x * 128 : OOB, v4u{bits, tag, bits, tag});
    }
    if (w == 3) {
      wait_until(tcom + TQ / 10);
      {
        int off[2]; v2u g[2];
        off[0] = OFF_Q + (b * 2048 + hkv * 256) * 8 + lo16; off[1] = off[0] + 1024;
        gather<2, PSLEEP>(ws, off, g, tag);
        *(LAS v2u*)(L + L_Q + lane * 8) = g[0]; *(LAS v2u*)(L + L_Q + 512 + lane * 8) = g[1];
        if (lane == 0) flag[1] = tag;
        lds_inc(L, 0, lane);
      }
      lds_wait_ge(flag + 3, 8u);
      const int h = g4 & 1;
      const v2f ml = *(const LAS v2f*)(L + L_ML + (h * 8 + lo3) * 8);
      float M = ml.x;
      M = fmaxf(M, dpp<XOR1>(M)); M = fmaxf(M, dpp<XOR2>(M)); M = fmaxf(M, dpp<XOR7>(M));
      float Ls = __builtin_amdgcn_exp2f(ml.x - M) * ml.y;
      Ls += dpp<XOR1>(Ls); Ls += dpp<XOR2>(Ls); Ls += dpp<XOR7>(Ls);
      const int mo = OFF_ML + (b * 8 + hkv) * 2048 + h * 16;
      stsc(ws, (lane == 0 || lane == 16) ? mo + s * 128 : OOB, v4u{__float_as_uint(M), tag, __float_as_uint(Ls), tag});
      waitvm<0>();
      lds_wait_ge(flag + 6, 1u);
      int off[2]; v2u g[2];
      off[0] = mo + n16 * 128; off[1] = lane < 16 ? OFF_PREF + (lane & 3) * 128 : OOB;
      gather<2, PSLEEP>(ws, off, g, tag);
      const unsigned nb = (unsigned)__ballot(((g[1].x >> (lane >> 3)) ^ (lane >> 2)) & 1) & 0xffffu;
      if (lane == 0) flag[8] = nb;
      float G = __uint_as_float(g[0].x);
      G = fmaxf(G, dpp<XOR1>(G)); G = fmaxf(G, dpp<XOR2>(G)); G = fmaxf(G, dpp<ROR4>(G)); G = fmaxf(G, dpp<ROR8>(G));
      float T = __builtin_amdgcn_exp2f(__uint_as_float(g[0].x) - G) * __uint_as_float(g[0].y);
      T += dpp<XOR1>(T); T += dpp<XOR2>(T); T += dpp<ROR4>(T); T += dpp<ROR8>(T);
      if (lane < 32 && !(lane & 8)) *(LAS float*)(L + L_FW + (h * 8 + lo3) * 4) = __builtin_amdgcn_exp2f(ml.x - G) / T;
      lds_inc(L, 5, lane);
    }
    unsigned gav = 0;
    const int cg = j * 8 + x;
    unsigned xin0 = 0, xin1 = 0;
    if (j < 7 && w == 0) {
      lds_wait_ge(flag + 6, 1u);
      xin0 = p.x[8 * cg + lo3]; xin1 = p.x[D + 8 * cg + lo3];
      gav = gather8(ws, OFF_MOD + (2 * D + 8 * cg + lo3) * 8, tag);
      PIN(xin0); PIN(xin1);
    }
    lds_wait_ge(flag + 5, 1u);
    const unsigned near = __builtin_amdgcn_readfirstlane(flag[8]);
    const int vd = (w * 16 + n16 < NCG) ? g4 * 8 * D * 2 + w * 256 + n16 * 16 : OOB;
    const rsrc_t rw = mkrsrc(roleF ? p.wd + (size_t)f * 64 * D : p.wo + (size_t)e * 32 * D, (roleF ? 64 : 32) * D * 2);
    const u16* gu = ((lane & 8) ? p.wu : p.wg) + (size_t)(32 + 8 * g4) * FF + (roleF ? f : 0) * 64 + lo3 * 8;
#define GU_PIECE(n_) load_to_lds((unsigned)(size_t)(L + L_W) + (56 + (n_)) * 1024, gu + (size_t)(64 * ((n_) >> 3) + ((n_) & 7)) * FF);
    if (roleS | roleF) {
      lds_wait_ge(flag + 4, 8u);
      if (roleS) {
#pragma unroll
        for (int r = 0; r < 8; ++r) wt[r] = ldnt(rw, vd, r * D * 2);
        SB();
      } else {
#pragma unroll 1
        for (int n = 0; n < NGT; ++n) GU_PIECE(4 * n + w)
      }
    }
    {
      const int h = w >> 1, dp = t & 127;
      v4f f0 = *(const LAS v4f*)(L + L_FW + h * 32), f1 = *(const LAS v4f*)(L + L_FW + h * 32 + 16);
      int po = po_base((2 * hkv + h) * 8 + (dp >> 4), near) + (b * 16 + s) * 256 + (dp & 15) * 16;
      PIN(f0); PIN(f1); PIN(po);
      wgbar();
      const LAS v2f* O = (const LAS v2f*)(L + L_V + h * 1024 + dp * 8);
      v2f v[8];
#pragma unroll
      for (int k = 0; k < 8; ++k) v[k] = O[k * 512];
      SB();
      const v2f o = ((v[0] * f0.x + v[1] * f0.y) + (v[2] * f0.z + v[3] * f0.w)) + ((v[4] * f1.x + v[5] * f1.y) + (v[6] * f1.z + v[7] * f1.w));
      stsc(ws, po, v4u{__float_as_uint(o.x), tag, __float_as_uint(o.y), tag});
      SB();
    }
    const int cg0 = 16 * w + 4 * g4;

    if (roleS) {
      int off[2]; v2u g[2];
      off[0] = po_base(e, near) + (w >> 1) * 4096 + hi3 * 256 + (w & 1) * 128 + lo3 * 16; off[1] = off[0] + 2048;
      int so = cg0 < NCG ? y1_line(cg0 + (hi3 & 1), e, near) + lo3 * 16 : OOB, so2 = cg0 < NCG ? y1_line(cg0 + 2 + (hi3 & 1), e, near) + lo3 * 16 : OOB;
      LAS u16* ow = (LAS u16*)(L + L_S1 + w * 32 + lo3 * 4 + (lane >> 5) * 2);
      const LAS v4u* orr = (const LAS v4u*)(L + L_S1 + (lane & 8) * 8 + g4 * 16);
      PIN(so); PIN(so2); PIN(ow); PIN(orr);
      waitvm<1>();
      {
        const v4u a = ldsc(ws, off[0]), c = ldsc(ws, off[1]);
        SB();
        tile_cols(wt, wc0);
        g[0] = v2u{a.x, a.z}; g[1] = v2u{c.x, c.z};
        if (!__all((a.y == tag) & (a.w == tag) & (c.y == tag) & (c.w == tag))) gather_all<2>(ws, off, g, tag);
      }
      float v = tr32(__uint_as_float(g[0].x) + __uint_as_float(g[1].x), __uint_as_float(g[0].y) + __uint_as_float(g[1].y));
      v = tr16(v, v); v += dpp<ROR8>(v);
      *ow = (u16)pk2(v, v);
      wgbar();
      const v4u ob = *orr;
      publish_tile(ws, tile_gemv<false>(wc0, ob, wc0, ob), so, so2, tag);
      asm volatile(".Lmk_end1:");
    } else if (roleF) {
#pragma unroll 1
      for (int n = NGT; n < 14; ++n) GU_PIECE(4 * n + w)
      lds_inc(L, 7, lane); lds_spin_ge(flag + 7, 4u);
#pragma unroll
      for (int r = 0; r < NDF; ++r) wt[r] = ldnt(rw, vd, ((r >> 3) * 32 + (r & 7)) * D * 2);
      SB();
      waitvm<NDF>();
      wgbar();
#pragma unroll
      for (int r = NDF; r < 16; ++r) wt[r] = ldnt(rw, vd, ((r >> 3) * 32 + (r & 7)) * D * 2);
      SB();
      int so = cg0 < NCG ? y2_line(cg0 + (hi3 & 1), f, near) + lo3 * 16 : OOB, so2 = cg0 < NCG ? y2_line(cg0 + 2 + (hi3 & 1), f, near) + lo3 * 16 : OOB;
      PIN(so); PIN(so2);
      tile_cols(wt, wc0);
      lds_wait_ge(flag + 2, 7u);
      v4f cg_ = Z4, cu_ = Z4;
#pragma unroll
      for (int k = 0; k < 7; ++k) { cg_ += *(const LAS v4f*)(L + L_V + k * 4096 + 1024 + lane * 32); cu_ += *(const LAS v4f*)(L + L_V + k * 4096 + 1024 + lane * 32 + 16); }
      PIN(cg_); PIN(cu_);
      tile_cols(wt + 8, wc1);
#pragma unroll 1
      for (unsigned spins = 0; spins < (SPIN_CAP << 5); ++spins) { if (__all(flag[9 + min(lo3, 6)] != 0u)) break; }
      asm volatile("" ::: "memory");
      SB();
      const LAS v4u* XR = (const LAS v4u*)(L + L_H + (lane >> 5) * 1024);
      const bool in = (lane & 31) < 28;
      const v4u xa = XR[in ? 2 * (lane & 31) : 0], xb = XR[in ? 2 * (lane & 31) + 1 : 0];
      const float pv = bflo(*(const LAS unsigned*)XR);
      v2f s1 = {0.f, 0.f}, s2 = {0.f, 0.f};
#pragma unroll
      for (int k = 0; k < 8; ++k) { const unsigned u = k < 4 ? xa[k & 3] : xb[k & 3]; const v2f d = v2f{bflo(u), bfhi(u)} - pv; s1 += d; s2 += d * d; }
      float q = tr16(in ? s1.x + s1.y : 0.f, in ? s2.x + s2.y : 0.f);
      q += dpp<ROR8>(q); q += dpp<ROR4>(q); q += dpp<XOR2>(q); q += dpp<XOR1>(q);
      q *= 1.f / D;
      const auto qq = __builtin_amdgcn_permlane16_swap(__float_as_uint(q), __float_as_uint(q), false, false);
      const float m1 = __uint_as_float(qq[0]), m2 = __uint_as_float(qq[1]);
      const float rs = __builtin_amdgcn_rcpf(rbf(__builtin_amdgcn_sqrtf(rbf(rbf(fmaxf(m2 - m1 * m1, 0.f)) + 1e-5f)))), mr = rbf(pv + m1) * rs;
      const auto r2 = __builtin_amdgcn_permlane32_swap(__float_as_uint(rs), __float_as_uint(rs), false, false);
      const auto mm = __builtin_amdgcn_permlane32_swap(__float_as_uint(mr), __float_as_uint(mr), false, false);
      float r0 = __uint_as_float(r2[0]), r1 = __uint_as_float(r2[1]);
      const float mr0 = __uint_as_float(mm[0]), mr1 = __uint_as_float(mm[1]);
      v4f off4 = {cg_.z - mr0 * cg_.x, cg_.w - mr1 * cg_.y, cu_.z - mr0 * cu_.x, cu_.w - mr1 * cu_.y};
      LAS u16* aw = (LAS u16*)(L + L_ACT + w * 256 + lo3 * 16 + hi3 * 2);
      const LAS v4u* E = (const LAS v4u*)(L + L_ACT + w * 256 + (lane & 8) * 16 + g4 * 16);
      const LAS v4f* GW = (const LAS v4f*)(L + L_GU + lo16);
      PIN(off4); PIN(r0); PIN(r1); PIN(aw); PIN(E); PIN(GW);
      wgbar();
      v4f gw[7];
#pragma unroll
      for (int k = 0; k < 7; ++k) gw[k] = GW[k * 64];
      SB();
      const v4f a = (((gw[0] + gw[1]) + (gw[2] + gw[3])) + ((gw[4] + gw[5]) + gw[6])) * v4f{r0, r1, r0, r1} + off4;
      const unsigned gb = pk2(a.x, a.y), ub = pk2(a.z, a.w), sg = pk2(silu(bflo(gb)), silu(bfhi(gb)));
      const unsigned ac = pk2(bflo(sg) * bflo(ub), bfhi(sg) * bfhi(ub));
      aw[0] = (u16)ac; aw[64] = (u16)(ac >> 16);
      asm volatile("" ::: "memory");
      const v4u e0 = E[0], e1 = E[4];
      publish_tile(ws, tile_gemv<true>(wc0, e0, wc1, e1), so, so2, tag);
      asm volatile(".Lmk_end2:");
    } else if (j < 7) {
      int of[4]; v2u gg[4];
#pragma unroll
      for (int k = 0; k < 4; ++k) of[k] = y1_line(cg, k * 32, near) + t * 16;
      LAS float* rw_ = (LAS float*)(L + L_RED + (w * 16 + (lane >> 5) * 8 + lo3) * 4);
      const LAS float* rr = (const LAS float*)(L + L_RED + n16 * 4);
      float gaf = (lane & 8) ? bfhi(gav) : bflo(gav), xf = bflo((lane & 8) ? xin1 : xin0);
      int xo = (lane < 32 && !(lane & 1)) ? OFF_X1 + (lane & 16) * 512 + cg * 128 + (n16 >> 1) * 8 : OOB;
      PIN(rw_); PIN(rr); PIN(gaf); PIN(xf); PIN(xo);
      gather_all<4>(ws, of, gg, tag);
      const float a0 = (__uint_as_float(gg[0].x) + __uint_as_float(gg[1].x)) + (__uint_as_float(gg[2].x) + __uint_as_float(gg[3].x));
      const float a1 = (__uint_as_float(gg[0].y) + __uint_as_float(gg[1].y)) + (__uint_as_float(gg[2].y) + __uint_as_float(gg[3].y));
      float v2 = tr32(a0, a1); v2 = tr16(v2, v2); v2 += dpp<ROR8>(v2);
      *rw_ = v2;
      wgbar();
      if (w == 0) {
        const float x1u = xf + rbf(gaf * rbf((rr[0] + rr[16]) + (rr[32] + rr[48])));
        stsc2(ws, xo, v2u{pk2(x1u, dpp<XOR1>(x1u)), tag});
        SB();
        const float x1o = dpp<ROR8>(x1u);
        float x1f = rbf(((lane >> 5) == (hi3 & 1)) ? x1u : x1o);
        int og[1]; v2u gv[1];
        og[0] = OFF_MOD + (5 * D + 8 * cg + (lo3 & 6)) * 8;
        gather<1, 4>(ws, og, gv, tag);
        const unsigned gbv = (lane & 1) ? gv[0].y : gv[0].x;
        float gbf = lane >= 32 ? bfhi(gbv) : bflo(gbv);
        const rsrc_t ro = mkrsrc(p.out, 2 * D * 2);
        int oo = (lane & 25) == 0 ? ((lane >> 5) * D + 8 * cg + lo3) * 2 : OOB;
        PIN(gbf); PIN(x1f); PIN(oo);
        int o2[4]; v2u g2[4];
#pragma unroll
        for (int k = 0; k < 4; ++k) o2[k] = (k * 8 + hi3 < N2) ? y2_line(cg, 0, near) + k * 1024 + lo16 : OOB;
        gather_all<4>(ws, o2, g2, tag, 0xffffffff00000000ull);
        const float c0 = (__uint_as_float(g2[0].x) + __uint_as_float(g2[1].x)) + (__uint_as_float(g2[2].x) + __uint_as_float(g2[3].x));
        const float c1 = (__uint_as_float(g2[0].y) + __uint_as_float(g2[1].y)) + (__uint_as_float(g2[2].y) + __uint_as_float(g2[3].y));
        float y2 = tr32(c0, c1); y2 = tr16(y2, y2); y2 += dpp<ROR8>(y2);
        const float res = x1f + rbf(gbf * rbf(y2));
        __builtin_amdgcn_raw_buffer_store_b32(pk2(res, dpp<XOR1>(res)), ro, oo, 0, 0);
        if (cg == 0 && lane == 0) __hip_atomic_store((GAS unsigned*)(p.ws + OFF_EPOCH), tag, __ATOMIC_RELAXED, __HIP_MEMORY_SCOPE_AGENT);
        asm volatile(".Lmk_end3:");
      }
    }
    if (j == 8 && x < 7) {
      constexpr int pitch = 6 * D * 2;
      const rsrc_t rm = mkrsrc(p.wmod, D * pitch);
      const int v1 = (128 * w + 8 * hi3) * pitch + (35 + x) * 128 + lo3 * 16, v2 = w < 3 ? v1 + 64 * pitch : OOB;
#pragma unroll
      for (int r = 0; r < 16; ++r) wt[r] = ldnt(rm, r < 8 ? v1 : v2, (r & 7) * pitch);
      SB();
      tile_cols(wt, wc0); tile_cols(wt + 8, wc1);
      UNIT_GEMV(35 + x, true, 8u)
      asm volatile(".Lmk_end4:");
    }
  } else {
    const int sw = w - 4;
    const rsrc_t rK = mkrsrc((const char*)p.K + kvb, 64 * DH * 2), rV = mkrsrc((const char*)p.V + kvb, 64 * DH * 2);
    const int vv = g4 * 512 + n16 * 16;
    v4u kv[64];
    const bool heavy = j < 16;
    const unsigned step0 = KB * (heavy ? (TEND - P0H) * 16 / 630 : TEND * 16 / 630);
    unsigned step = step0, due = (heavy ? P0H * 16 / 10 : 0) + sw * (step0 >> 3);
    int applied = 0;
#define PACE(i) if ((i) % KB == 0) pace(due, step, tk0);
#define ALIGN() { const int c_ = __builtin_amdgcn_readfirstlane(*(volatile LAS int*)(L + L_VF)); \
      if (c_ != NOSHIFT) { due += c_ - applied; applied = c_; step = step0 + (c_ & 1) * (KB * COLDX * 16 / 540); } }
    const int rot = sw + j;
#define KSEG(st) ((((st) + rot * KB) & 3) * 128)
#define VSEG(i) ((((i) + rot) & 1) * 256)
#define PARK(i) ((i) < 4 ? L_V + sw * 4096 + (i) * 1024 : L_W + (56 + sw * 7 + (i) - 4) * 1024)
#define LOADKV(i) kv[i] = (i) < 32 ? ldnt(rK, vk, ((i) >> 2) * 4096 + KSEG(i)) : ldnt(rV, vv, (((i) - 32) >> 1) * 2048 + VSEG(i))
#define ISSUE_TO(FROM, TO) _Pragma("unroll") for (int i_ = (FROM); i_ < (TO); ++i_) { PACE(i_) LOADKV(i_); SB(); }
    if (j == 31 && sw == 7) {
      u64 t1, t2; unsigned ep;
      asm volatile("s_load_dword %2, %3, 0x0\n\ts_waitcnt lgkmcnt(0)\n\ts_mov_b64 %0, %4\n\ts_memrealtime %1\n\ts_waitcnt lgkmcnt(0)" : "=&s"(t1), "=&s"(t2), "=&s"(ep) : "s"(p.ws), "s"(tk64) : "memory");
      const unsigned tg = ep + 1u ? ep + 1u : 1u, ta = ((unsigned)t1 & ~1u) | ((unsigned)(t2 - t1) > COLDT / 10);
      stsc(mkrsrc(p.ws, WS_BYTES), lane < 3 ? OFF_T0 + lane * 1024 + x * 128 : OOB, v4u{ta, tg, ta, tg});
      SB();
    }
    if (!heavy) {
#pragma unroll
      for (int i = 0; i < KB; ++i) { LOADKV(i); SB(); }
    }
    ROLES()
    if ((roleA | roleB | roleF) && sw < 7) {
      const int wp = roleB ? HQ * DH : roleA ? 6 * D : FF;
      const u16* src = roleF ? ((lane & 8) ? p.wu : p.wg) + (size_t)(64 * sw + 8 * g4) * FF + f * 64 + lo3 * 8
                             : (roleB ? p.wq : p.wmod) + (size_t)(64 * sw + 8 * hi3) * wp + unit * 64 + lo3 * 8;
#pragma unroll
      for (int r = 0; r < 8; ++r) load_to_lds((unsigned)(size_t)(L + L_W) + (sw * 8 + r) * 1024, src + (size_t)r * wp);
    }
    if (heavy) {
#pragma unroll
      for (int i = 0; i < KB; ++i) { LOADKV(i); SB(); }
      waitvm<KB>();
    }
    wgbar();
#pragma unroll
    for (int i = KB; i < 32; ++i) {
      if (i == 2 || i == 4 || i == 6 || i == 10 || i == 12 || i == 14 || i == 18 || i == 22) ALIGN()
      waitvm<TD - 1>(); PACE(i) LOADKV(i); SB();
    }
    {
      const unsigned lb = (unsigned)(size_t)L;
#pragma unroll
      for (int i = 0; i < NVL; ++i) {
        waitvm<TD - 1>();
        PACE(32 + i)
        unsigned keep;
        asm volatile("s_mov_b32 %0, m0\n\ts_mov_b32 m0, %1\n\ts_nop 0\n\tbuffer_load_dwordx4 %2, %3, %4 offen nt lds\n\ts_mov_b32 m0, %0"
                     : "=&s"(keep) : "s"(lb + PARK(i)), "v"(vv), "s"(rV), "s"((i >> 1) * 2048 + VSEG(i)) : "memory");
        SB();
      }
    }
    lds_wait_ge(flag, 1u);
    v4u qa[4];
#pragma unroll
    for (int st = 0; st < 4; ++st) qa[st] = *(const LAS v4u*)(L + L_Q + (lane & 1) * 512 + KSEG(st) + (lane & 2) * 32 + g4 * 16);
    ALIGN()
    float ev0 = 0.f, ev1 = 0.f, ov0 = 0.f, ov1 = 0.f;
    const bool c2 = lane & 8;
#pragma unroll
    for (int tt = 0; tt < 8; ++tt) {
      v4f a = Z4;
#pragma unroll
      for (int st = 0; st < 4; ++st) {
        const int n = tt * 4 + st;
        ISSUE_TO(n == 0 ? 32 + NVL : issued_before(n - 1), issued_before(n))
        a = mfma(qa[st], kv[n], a);
        SB();
      }
      const float v0 = c2 ? a.z : a.x, v1 = c2 ? a.w : a.y;
      const bool mine = g4 == (tt >> 1);
      if (tt & 1) { ov0 = mine ? v0 : ov0; ov1 = mine ? v1 : ov1; } else { ev0 = mine ? v0 : ev0; ev1 = mine ? v1 : ev1; }
    }
    const float sc0 = rbf(trd<ROR8>(ev0, ov0, c2)) * (0.0625f * 1.4426950408889634f);
    const float sc1 = rbf(trd<ROR8>(ev1, ov1, c2)) * (0.0625f * 1.4426950408889634f);
    {
      auto sx = __builtin_amdgcn_permlane32_swap(__float_as_uint(sc0), __float_as_uint(sc1), false, false);
      float m = fmaxf(__uint_as_float(sx[0]), __uint_as_float(sx[1]));
      m = max16(m); m = fmaxf(m, dpp<ROR8>(m)); m = fmaxf(m, dpp<ROR4>(m)); m = fmaxf(m, dpp<XOR2>(m)); m = fmaxf(m, dpp<XOR1>(m));
      const float m0 = __builtin_bit_cast(float, __builtin_amdgcn_readlane(__builtin_bit_cast(int, m), 0));
      const float m1 = __builtin_bit_cast(float, __builtin_amdgcn_readlane(__builtin_bit_cast(int, m), 32));
      const float p0 = __builtin_amdgcn_exp2f(sc0 - m0), p1 = __builtin_amdgcn_exp2f(sc1 - m1);
      float l = tr32(p0, p1);
      l = tr16(l, l); l += dpp<ROR8>(l); l += dpp<ROR4>(l); l += dpp<XOR2>(l); l += dpp<XOR1>(l);
      const unsigned pp = pk2(p0, p1);
      LAS unsigned* P = (LAS unsigned*)(L + L_P + sw * 1024 + (lane & 3) * 64 + (lane >> 2) * 4);
      P[0] = pp & 0xffffu; P[64] = pp << 16; P[128] = pp >> 16; P[192] = pp & 0xffff0000u;
      if ((lane & 31) == 0) *(LAS v2f*)(L + L_ML + ((lane >> 5) * 8 + sw) * 8) = v2f{m, l};
    }
    lds_inc(L, 3, lane);
    waitvm<issued_before(31) - 32 - NVL>();
#pragma unroll
    for (int i = 0; i < NVL; ++i) kv[32 + i] = *(const LAS v4u*)(L + PARK(i) + lo16);
    v4u pr[4];
#pragma unroll
    for (int k = 0; k < 4; ++k) pr[k] = *(const LAS v4u*)(L + L_P + sw * 1024 + (lane & 8) * 64 + (lane & 1) * 256 + g4 * 64 + k * 16);
    const rsrc_t ws = mkrsrc(p.ws, WS_BYTES);
    int om[2];
    om[0] = (roleF && sw < 7) ? OFF_MOD + (3 * D + 64 * sw + 8 * hi3 + 2 * (lane & 3)) * 8 : OOB; om[1] = (roleF && sw < 7) ? om[0] + D * 8 : OOB;
    const v4u ms = ldsc(ws, om[0]), mc = ldsc(ws, om[1]);
    SB();
    v4f acc0 = Z4, acc1 = Z4;
    const int jq = (lane >> 1) & 3;
#pragma unroll
    for (int ip = 0; ip < 16; ++ip) {
      ISSUE_TO(issued_before(32 + 2 * ip - 1), issued_before(32 + 2 * ip + 1))
      if (ip == ML_AT) lds_inc(L, 6, lane);
      if (issued_before(32 + 2 * ip - 1) < 64 && issued_before(32 + 2 * ip + 1) == 64) lds_inc(L, 4, lane);
      const unsigned pu = pr[ip >> 2][ip & 3];
      const v4u pa = {jq == 0 ? pu : 0u, jq == 1 ? pu : 0u, jq == 2 ? pu : 0u, jq == 3 ? pu : 0u};
      acc0 = mfma(pa, kv[32 + 2 * ip], acc0); acc1 = mfma(pa, kv[33 + 2 * ip], acc1);
      SB();
    }
    LAS v4f* O = (LAS v4f*)(L + L_V + sw * 4096 + (g4 >> 1) * 1024 + n16 * 32 + (g4 & 1) * 16);
    O[VSEG(0) / 8] = acc0; O[VSEG(1) / 8] = acc1;
    wgbar();
    if (roleF && sw < 7) {
      const unsigned tag = __builtin_amdgcn_readfirstlane(flag[1]);
      const bool hb = lane & 4;
      v2u gm[2] = {{ms.x, ms.z}, {mc.x, mc.z}};
      if (!__all((ms.y == tag) & (ms.w == tag) & (mc.y == tag) & (mc.w == tag))) gather<2>(ws, om, gm, tag);
      const unsigned cbp = hb ? phi(gm[1].x, gm[1].y) : plo(gm[1].x, gm[1].y), cb1 = pk2(1.f + bflo(cbp), 1.f + bfhi(cbp));
      LAS unsigned* Z = (LAS unsigned*)(L + L_V + sw * 4096 + 3072 + (lane & 4) * 32 + hi3 * 16 + (lane & 3) * 4);
      const LAS v4u* ZA = (const LAS v4u*)(L + L_V + sw * 4096 + 3072 + (lane & 3) * 128 + g4 * 16);
      Z[0] = cb1; Z[64] = hb ? phi(gm[0].x, gm[0].y) : plo(gm[0].x, gm[0].y);
      asm volatile("" ::: "memory");
      v4u wt[8], gc0[8], gc1[8];
      v4f y[8];
#pragma unroll
      for (int k = 0; k < 8; ++k) wt[k] = *(const LAS v4u*)(L + L_W + (sw * 8 + k) * 1024 + lo16);
      tile_cols(wt, gc0);
      {
        const v4u h0 = ZA[0], h1 = ZA[4];
#pragma unroll
        for (int c = 0; c < 8; ++c) { y[c] = mfma(h0, gc0[c], Z4); PIN(y[c]); }
        wgbar();
#pragma unroll
        for (int k = 0; k < 8; ++k) wt[k] = *(const LAS v4u*)(L + L_W + (56 + sw * 8 + k) * 1024 + lo16);
        tile_cols(wt, gc1);
#pragma unroll
        for (int c = 0; c < 8; ++c) y[c] = mfma(h1, gc1[c], y[c]);
        if (lane < 16) {
          LAS v4f* G = (LAS v4f*)(L + L_V + sw * 4096 + 1024 + lo3 * 32 + (lane & 8) * 2);
#pragma unroll
          for (int c = 0; c < 8; ++c) G[16 * c] = y[c];
        }
      }
      float cl = bflo(cb1), ch = bfhi(cb1);
      int ox = OFF_X1 + ((__builtin_amdgcn_readfirstlane(flag[8]) >> x) & 1) * 8192 + (8 * sw + hi3) * 128 + lo3 * 8;
      LAS v2f* GO = (LAS v2f*)(L + L_GU + sw * 1024 + lo3 * 16 + (lane & 8));
      LAS unsigned* XC = (LAS unsigned*)(L + L_H + (lane & 4) * 256 + sw * 128 + hi3 * 16 + (lane & 3) * 4);
      PIN(cl); PIN(ch); PIN(ox); PIN(XC); PIN(GO);
      lds_inc(L, 2, lane);
      const unsigned xg = gather8(ws, ox, tag);
      const float x0 = bflo(xg), x1 = bfhi(xg), z0 = x0 * cl, z1 = x1 * ch;
      const unsigned zh = pk2(z0, z1);
      Z[0] = zh; Z[64] = pk2(z0 - bflo(zh), z1 - bfhi(zh));
      asm volatile("" ::: "memory");
      const v4u h0 = ZA[0], h1 = ZA[4];
      asm volatile("" ::: "memory");
      *XC = xg;
      asm volatile("" ::: "memory");
      flag[9 + sw] = 1u;
      SB();
#pragma unroll
      for (int c = 0; c < 8; ++c) y[c] = mfma(h0, gc0[c], Z4);
      SB();
#pragma unroll
      for (int c = 0; c < 8; ++c) y[c] = mfma(h1, gc1[c], y[c]);
      if (lane < 16) {
#pragma unroll
        for (int c = 0; c < 8; ++c) GO[16 * c] = v2f{y[c].x + y[c].z, y[c].y + y[c].w};
      }
      wgbar();
    }
  }
}

extern "C" void kernel_launch(void* const* d_in, const int* in_sizes, int n_in, void* d_out, int out_size, void* d_ws, size_t ws_size, hipStream_t stream) {
  Params p;
  p.x = (const u16*)d_in[0]; p.K = (const u16*)d_in[1]; p.V = (const u16*)d_in[2]; p.wq = (const u16*)d_in[3]; p.wo = (const u16*)d_in[4];
  p.wg = (const u16*)d_in[6]; p.wu = (const u16*)d_in[7]; p.wd = (const u16*)d_in[8]; p.c = (const u16*)d_in[9]; p.wmod = (const u16*)d_in[10];
  p.out = (u16*)d_out; p.ws = (unsigned char*)d_ws;
  (void)hipFuncSetAttribute((const void*)mk_kernel, hipFuncAttributeMaxDynamicSharedMemorySize, LDS_BYTES);
  hipLaunchKernelGGL(mk_kernel, dim3(NWG), dim3(NT), LDS_BYTES, stream, p);
}
```
